```python
import torch

def _softmax(x, axis=-1):
    return torch.softmax(x, dim=axis)
def _gelu(x):
    return torch.nn.functional.gelu(x, approximate="tanh")
def _silu(x):
    return torch.nn.functional.silu(x)
def _sigmoid(x):
    return torch.sigmoid(x)
def _layernorm(x, axis=-1, eps=1e-5):
    m = x.mean(dim=axis, keepdim=True)
    v = x.var(dim=axis, keepdim=True, unbiased=False)
    return (x - m) / torch.sqrt(v + eps)
def _scatter_add(tgt, idx, src, axis=0):
    return tgt.clone().index_add_(axis, idx, src)
def _segment_sum(src, seg, n):
    out = torch.zeros((n,) + src.shape[1:], dtype=src.dtype)
    return out.index_add_(0, seg, src)
def _pad(x, pw):
    flat = tuple(v for pair in reversed(pw) for v in pair)
    return torch.nn.functional.pad(x, flat)

kernel_name = 'gated_mix__softmax_full_mha__softmax_full_mha__prompt'

def setup_inputs():

    _w = (lambda a: (lambda t: t.to(torch.bfloat16) if t.is_floating_point() else t)(torch.as_tensor(a)))
    def _A_setup():
        B, S, D = 1, 2048, 448
        return {
            "x": _w((lambda *s: torch.randn(*s, dtype=torch.bfloat16) * 0.05)(B, S, D)),
            "wqkv": _w((lambda *s: torch.randn(*s, dtype=torch.bfloat16) * 0.8)(D, 3 * D)),
            "wo": _w((lambda *s: torch.randn(*s, dtype=torch.bfloat16) * 0.05)(D, D)),
        }
    _A_in = _A_setup()

    def _B_setup():
        B, S, D = 1, 2048, 448
        return {
            "x": _w((lambda *s: torch.randn(*s, dtype=torch.bfloat16) * 0.05)(B, S, D)),
            "wqkv": _w((lambda *s: torch.randn(*s, dtype=torch.bfloat16) * 0.8)(D, 3 * D)),
            "wo": _w((lambda *s: torch.randn(*s, dtype=torch.bfloat16) * 0.05)(D, D)),
        }
    _B_in = _B_setup()

    return {
        "A_x": _A_in["x"],
        "A_wqkv": _A_in["wqkv"],
        "A_wo": _A_in["wo"],
        "B_x": _B_in["x"],
        "B_wqkv": _B_in["wqkv"],
        "B_wo": _B_in["wo"],
        "gate_w": _w((lambda *s: torch.randn(*s, dtype=torch.bfloat16) * 0.05)(448) * 16.0),
    }

def reference(A_x, A_wqkv, A_wo, B_x, B_wqkv, B_wo, gate_w):
    def _A_ref(x, wqkv, wo):
        B, S, D = x.shape
        H = 16; Dh = D // H
        qkv = (lambda x, *d: x.permute(*d))((x @ wqkv).reshape(B, S, 3, H, Dh), 2, 0, 3, 1, 4)
        q, k, v = qkv[0], qkv[1], qkv[2]
        scale = Dh ** -0.5
        scores = torch.einsum("bhqd,bhkd->bhqk", q, k) * scale
        p = _softmax(scores, axis=-1)
        o = torch.einsum("bhqk,bhkd->bhqd", p, v)
        return (lambda x, *d: x.permute(*d))(o, 0, 2, 1, 3).reshape(B, S, D) @ wo

    def _B_ref(x, wqkv, wo):
        B, S, D = x.shape
        H = 16; Dh = D // H
        qkv = (lambda x, *d: x.permute(*d))((x @ wqkv).reshape(B, S, 3, H, Dh), 2, 0, 3, 1, 4)
        q, k, v = qkv[0], qkv[1], qkv[2]
        scale = Dh ** -0.5
        scores = torch.einsum("bhqd,bhkd->bhqk", q, k) * scale
        p = _softmax(scores, axis=-1)
        o = torch.einsum("bhqk,bhkd->bhqd", p, v)
        return (lambda x, *d: x.permute(*d))(o, 0, 2, 1, 3).reshape(B, S, D) @ wo

    x = A_x
    g = _sigmoid((x * gate_w).sum(-1, keepdims=True))
    a = _A_ref(x, A_wqkv, A_wo)
    b = _B_ref(x, B_wqkv, B_wo)
    return g * a + (1 - g) * b
```

```cpp
#include <hip/hip_runtime.h>
#include <hip/hip_bf16.h>
#include <stdint.h>

typedef __attribute__((ext_vector_type(8))) __bf16 bf16x8;
typedef __attribute__((ext_vector_type(16))) __bf16 bf16x16;
typedef __attribute__((ext_vector_type(16))) float f32x16;
typedef __attribute__((ext_vector_type(8))) float f32x8;
typedef __attribute__((ext_vector_type(4))) float f32x4;
typedef __attribute__((ext_vector_type(4))) unsigned u32x4;
typedef __attribute__((ext_vector_type(2))) unsigned u32x2;
typedef short v4i16_t __attribute__((ext_vector_type(4)));
#define LAS __attribute__((address_space(3)))
#define GAS __attribute__((address_space(1)))

constexpr int S_ = 2048, D_ = 448, DH = 28, N3 = 1344;
constexpr float QSCALE = 0.18898223650461362f * 1.4426950408889634f;

constexpr size_t WS_KV = 0;
constexpr size_t WS_Q = WS_KV + (8u << 20);
constexpr size_t WS_O = WS_Q + (4u << 20);
constexpr size_t WS_G = WS_O + (4u << 20);

typedef __attribute__((ext_vector_type(2))) float f32x2;
typedef __attribute__((ext_vector_type(2))) __bf16 bf16x2;
__device__ __forceinline__ unsigned cvtpk(float lo, float hi) {
    return __builtin_bit_cast(unsigned, __builtin_convertvector((f32x2){lo, hi}, bf16x2));
}
__device__ __forceinline__ f32x16 mfma32(u32x4 a, u32x4 b, f32x16 c) {
    return __builtin_amdgcn_mfma_f32_32x32x16_bf16(__builtin_bit_cast(bf16x8, a), __builtin_bit_cast(bf16x8, b), c, 0, 0, 0);
}
__device__ __forceinline__ f32x16 smfma32(u32x4 a, f32x8 b, f32x16 c, int idx) {
    return __builtin_amdgcn_smfmac_f32_32x32x32_bf16(__builtin_bit_cast(bf16x8, a), __builtin_bit_cast(bf16x16, b), c, idx, 0, 0);
}
__device__ __forceinline__ u32x2 tr_read(const LAS char* p) {
    return __builtin_bit_cast(u32x2, __builtin_amdgcn_ds_read_tr16_b64_v4i16((LAS v4i16_t*)p));
}
__device__ __forceinline__ u32x4 lds_read16(const LAS char* p) { return *(const LAS u32x4*)p; }
__device__ __forceinline__ void lds_write16(LAS char* p, u32x4 v) { *(LAS u32x4*)p = v; }
__device__ __forceinline__ int swap23(int i) { return (i & 0x13) | ((i & 4) << 1) | ((i & 8) >> 1); }

constexpr int W_SEC = D_ * 64;
constexpr int P1_XST = 3 * W_SEC;
constexpr int P1_LDS = P1_XST + 8 * 8192;

__global__ __launch_bounds__(512, 2) void k_qkv(const uint16_t* __restrict__ x, const uint16_t* __restrict__ wA, const uint16_t* __restrict__ wB,
                                               const uint16_t* __restrict__ gw, char* __restrict__ ws) {
    extern __shared__ __attribute__((aligned(16))) char smem[];
    LAS char* lds = (LAS char*)smem;
    const int tid = threadIdx.x, lane = tid & 63, wid = __builtin_amdgcn_readfirstlane(tid >> 6);
    const int xcd = blockIdx.x & 7, idx = blockIdx.x >> 3;
    const int hb = xcd * 4 + (idx >> 3), r = idx & 7;
    const int h = hb & 15;
    const uint16_t* w = (hb < 16) ? wA : wB;
    const __amdgpu_buffer_rsrc_t wr = __builtin_amdgcn_make_buffer_rsrc((void*)w, 0, D_ * N3 * 2, 0x00020000);
    {
        const int rowi = lane >> 2, ch = lane & 3;
        const unsigned voff = (unsigned)(rowi * (N3 * 2) + h * (DH * 2) + ch * 16);
#pragma unroll
        for (int i = 0; i < 11; ++i) {
            const int n = wid + 8 * i;
            if (n < 84) {
                const int sec = n / 28, kb = n % 28;
                const int colsec = (sec == 0) ? 1 : (sec == 1 ? 2 : 0);
                const unsigned soff = (unsigned)(kb * 16 * (N3 * 2) + colsec * (D_ * 2));
                if (sec < 2 || ch != 3)
                    __builtin_amdgcn_raw_ptr_buffer_load_lds(wr, lds + sec * W_SEC + kb * 1024, 16, voff, soff, 0, 0);
            }
        }
    }
    u32x2 wq_tail; unsigned gwv = 0;
    if (tid < D_) {
        wq_tail = *(const u32x2*)(w + (size_t)tid * N3 + h * DH + 24);
        gwv = gw[tid];
    }
    const uint16_t* xw = x + (size_t)(r * 256 + wid * 32) * D_;
    u32x4 xr[28];
#pragma unroll
    for (int n = 0; n < 28; ++n) {
        const int s = n >> 2, row = 8 * (n & 3) + (lane >> 3);
        xr[n] = *(const u32x4*)(xw + row * D_ + s * 64 + (lane & 7) * 8);
    }
    if (tid < D_) lds_write16(lds + 2 * W_SEC + tid * 64 + 48, (u32x4){wq_tail[0], wq_tail[1], gwv, 0u});
    asm volatile("s_waitcnt vmcnt(28) lgkmcnt(0)\n\ts_barrier" ::: "memory");

    LAS char* xst = lds + P1_XST + wid * 8192;
    const int r32 = lane & 31, hi = lane >> 5;
    const int wr_off = (lane >> 3) * 128;
    const LAS char* wfb = lds + (8 * hi + ((lane & 15) >> 2)) * 64 + ((lane >> 4) & 1) * 32 + (lane & 3) * 8;
    f32x16 aK = {}, aV = {}, aQ = {};
#pragma unroll
    for (int s = 0; s < 7; ++s) {
        LAS char* xs = xst + (s & 1) * 4096;
#pragma unroll
        for (int q = 0; q < 4; ++q) {
            const int row = 8 * q + (lane >> 3);
            lds_write16(xs + q * 1024 + wr_off + (((lane & 7) ^ ((row >> 1) & 7)) << 4), xr[4 * s + q]);
        }
        u32x4 xf[4];
#pragma unroll
        for (int ks = 0; ks < 4; ++ks) xf[ks] = lds_read16(xs + r32 * 128 + (((2 * ks + hi) ^ ((r32 >> 1) & 7)) << 4));
#pragma unroll
        for (int ks = 0; ks < 4; ++ks) {
            const int k0 = s * 64 + ks * 16;
            u32x2 a0 = tr_read(wfb + k0 * 64), a1 = tr_read(wfb + k0 * 64 + 256);
            u32x2 b0 = tr_read(wfb + W_SEC + k0 * 64), b1 = tr_read(wfb + W_SEC + k0 * 64 + 256);
            u32x2 c0 = tr_read(wfb + 2 * W_SEC + k0 * 64), c1 = tr_read(wfb + 2 * W_SEC + k0 * 64 + 256);
            aK = mfma32((u32x4){a0[0], a0[1], a1[0], a1[1]}, xf[ks], aK);
            aV = mfma32(xf[ks], (u32x4){b0[0], b0[1], b1[0], b1[1]}, aV);
            aQ = mfma32((u32x4){c0[0], c0[1], c1[0], c1[1]}, xf[ks], aQ);
        }
    }
    const int T = r * 8 + wid;
    char* kv = ws + WS_KV + ((size_t)(hb * 64 + T) << 12);
    char* qo = ws + WS_Q + ((size_t)(hb * 64 + T) << 11);
    {
        u32x4 k0 = {cvtpk(aK[0], aK[1]), cvtpk(aK[2], aK[3]), cvtpk(aK[4], aK[5]), cvtpk(aK[6], aK[7])};
        u32x4 k1 = {cvtpk(aK[8], aK[9]), cvtpk(aK[10], aK[11]), cvtpk(aK[12], aK[13]), cvtpk(aK[14], aK[15])};
        if (hi) { k1[2] = 0x3f803f80u; k1[3] = 0x00003f80u; }
        const int p = swap23(r32) + 32 * hi;
        *(u32x4*)(kv + p * 16) = k0;
        *(u32x4*)(kv + 1024 + p * 16) = k1;
        u32x4 v0 = {cvtpk(aV[0], aV[1]), cvtpk(aV[2], aV[3]), cvtpk(aV[4], aV[5]), cvtpk(aV[6], aV[7])};
        u32x4 v1 = {cvtpk(aV[8], aV[9]), cvtpk(aV[10], aV[11]), cvtpk(aV[12], aV[13]), cvtpk(aV[14], aV[15])};
        if (r32 >= DH) { const unsigned f = (r32 == DH) ? 0x3f803f80u : 0u; v0 = (u32x4){f, f, f, f}; v1 = v0; }
        *(u32x4*)(kv + 2048 + lane * 16) = v0;
        *(u32x4*)(kv + 3072 + lane * 16) = v1;
        const float glogit = aQ[12];
        f32x16 q = aQ * QSCALE;
        u32x4 q0 = {cvtpk(q[0], q[1]), cvtpk(q[2], q[3]), cvtpk(q[4], q[5]), cvtpk(q[6], q[7])};
        u32x4 q1 = {cvtpk(q[8], q[9]), cvtpk(q[10], q[11]), cvtpk(q[12], q[13]), cvtpk(q[14], q[15])};
        if (hi) { q1[2] = 0u; q1[3] = 0u; }
        *(u32x4*)(qo + lane * 16) = q0;
        *(u32x4*)(qo + 1024 + lane * 16) = q1;
        if (hb == 0 && hi) ((float*)(ws + WS_G))[T * 32 + r32] = 1.f / (1.f + __expf(-glogit));
    }
}

constexpr int CH = 32768;
constexpr int P2_LDS = 4 * CH;

__device__ __forceinline__ float xhalf_max(float v) {
    auto rr = __builtin_amdgcn_permlane32_swap(__float_as_uint(v), __float_as_uint(v), false, false);
    return fmaxf(__uint_as_float(rr[0]), __uint_as_float(rr[1]));
}
__device__ __forceinline__ float max16(const f32x16& s) {
    float a = fmaxf(fmaxf(s[0], s[1]), s[2]);
#pragma unroll
    for (int i = 3; i < 15; i += 2) a = fmaxf(fmaxf(a, s[i]), s[i + 1]);
    return fmaxf(a, s[15]);
}
__device__ __forceinline__ void set_shift(u32x4& q1, float m, int hi) {
    const float nm = -m;
    const unsigned h0 = cvtpk(nm, 0.f) & 0xffffu; const float f0 = __uint_as_float(h0 << 16);
    const float r1 = nm - f0;
    const unsigned h1 = cvtpk(r1, 0.f) & 0xffffu; const float f1 = __uint_as_float(h1 << 16);
    const float r2 = r1 - f1;
    const unsigned h2 = cvtpk(r2, 0.f) & 0xffffu;
    if (hi) { q1[2] = h0 | (h1 << 16); q1[3] = h2; }
}
__device__ __forceinline__ f32x8 lo8(const f32x16& v) { return __builtin_shufflevector(v, v, 0, 1, 2, 3, 4, 5, 6, 7); }
__device__ __forceinline__ f32x8 hi8(const f32x16& v) { return __builtin_shufflevector(v, v, 8, 9, 10, 11, 12, 13, 14, 15); }

__global__ __launch_bounds__(512, 2) void k_attn(char* __restrict__ ws) {
    extern __shared__ __attribute__((aligned(16))) char smem[];
    LAS char* lds = (LAS char*)smem;
    const int tid = threadIdx.x, lane = tid & 63, wid = __builtin_amdgcn_readfirstlane(tid >> 6);
    const int xcd = blockIdx.x & 7, idx = blockIdx.x >> 3;
    const int hb = xcd * 4 + (idx >> 3), r = idx & 7;
    const int r32 = lane & 31, hi = lane >> 5;
    const int T = r * 8 + wid;
    const char* kvh = ws + WS_KV + ((size_t)hb << 18);
    const char* src = kvh + wid * 4096 + lane * 16;
    auto issue = [&](int c) {
        const int cc = (c + r) & 7;
#pragma unroll
        for (int i = 0; i < 4; ++i)
            __builtin_amdgcn_global_load_lds((const GAS void*)(src + cc * CH + i * 1024), lds + (c & 3) * CH + wid * 4096 + i * 1024, 16, 0, 0);
    };
    const char* qo = ws + WS_Q + ((size_t)(hb * 64 + T) << 11);
    u32x4 q0 = *(const u32x4*)(qo + lane * 16), q1 = *(const u32x4*)(qo + 1024 + lane * 16);
    const float g = ((const float*)(ws + WS_G))[T * 32 + r32];
    asm volatile("" :: "v"(q0), "v"(q1), "v"(g));
    issue(0); issue(1); issue(2);
    asm volatile("s_waitcnt vmcnt(8)\n\ts_barrier" ::: "memory");
    const LAS char* lb = lds + lane * 16;
    int sidx = (int)0xdddddddd; asm volatile("" : "+v"(sidx));
    {
        f32x16 s = mfma32(lds_read16(lb), q0, f32x16{});
        s = mfma32(lds_read16(lb + 1024), q1, s);
        set_shift(q1, xhalf_max(max16(s)), hi);
    }
    f32x16 o = {};
    for (int c = 0; c < 8; ++c) {
        if (c < 6) asm volatile("s_waitcnt vmcnt(4) lgkmcnt(0)\n\ts_barrier" ::: "memory");
        else asm volatile("s_waitcnt vmcnt(0) lgkmcnt(0)\n\ts_barrier" ::: "memory");
        if (c < 5) issue(c + 3);
        const LAS char* cb = lb + (c & 3) * CH;
#pragma unroll
        for (int t = 0; t < 8; ++t) {
            const u32x4 k0 = lds_read16(cb + t * 4096), k1 = lds_read16(cb + t * 4096 + 1024);
            const u32x4 v0 = lds_read16(cb + t * 4096 + 2048), v1 = lds_read16(cb + t * 4096 + 3072);
            f32x16 s = mfma32(k0, q0, f32x16{});
            s = mfma32(k1, q1, s);
#pragma unroll
            for (int i = 0; i < 16; ++i) s[i] = __builtin_amdgcn_exp2f(s[i]);
            o = smfma32(v0, lo8(s), o, sidx);
            o = smfma32(v1, hi8(s), o, sidx);
        }
    }
    float l;
    { auto rr = __builtin_amdgcn_permlane32_swap(__float_as_uint(o[12]), __float_as_uint(o[12]), false, false); l = __uint_as_float(rr[1]); }
    const float sc = ((hb < 16) ? g : 1.f - g) / l;
    o = o * sc;
    u32x4 o0 = {cvtpk(o[0], o[1]), cvtpk(o[2], o[3]), cvtpk(o[4], o[5]), cvtpk(o[6], o[7])};
    u32x4 o1 = {cvtpk(o[8], o[9]), cvtpk(o[10], o[11]), cvtpk(o[12], o[13]), cvtpk(o[14], o[15])};
    if (hi) { o1[2] = 0u; o1[3] = 0u; }
    char* oo = ws + WS_O + ((size_t)(T * 32 + hb) << 11);
    *(u32x4*)(oo + lane * 16) = o0;
    *(u32x4*)(oo + 1024 + lane * 16) = o1;
}

constexpr int P3_LDS = 131072;
__global__ __launch_bounds__(512, 2) void k_out(const uint16_t* __restrict__ woA, const uint16_t* __restrict__ woB, const char* __restrict__ ws, uint16_t* __restrict__ out) {
    extern __shared__ __attribute__((aligned(16))) char smem[];
    LAS char* lds = (LAS char*)smem;
    const int tid = threadIdx.x, lane = tid & 63, wid = __builtin_amdgcn_readfirstlane(tid >> 6);
    const int xcd = blockIdx.x & 7, idx = blockIdx.x >> 3;
    const int tp = xcd * 4 + idx / 7, cg = idx % 7;
    const int r32 = lane & 31, hi = lane >> 5;
    const uint16_t* wo = (wid < 4) ? woA : woB;
    const __amdgpu_buffer_rsrc_t wr = __builtin_amdgcn_make_buffer_rsrc((void*)wo, 0, D_ * D_ * 2, 0x00020000);
    LAS char* wl = lds + wid * 16384;
    {
        const unsigned voff = (unsigned)(((wid & 3) * 112 + (lane >> 3)) * (D_ * 2) + cg * 128 + (lane & 7) * 16);
#pragma unroll
        for (int i = 0; i < 15; ++i) __builtin_amdgcn_raw_ptr_buffer_load_lds(wr, wl + i * 1024, 16, voff, (unsigned)(i * 8 * D_ * 2), 0, 0);
    }
    u32x4 of[2][8];
#pragma unroll
    for (int tt = 0; tt < 2; ++tt)
#pragma unroll
        for (int i = 0; i < 8; ++i) of[tt][i] = *(const u32x4*)(ws + WS_O + ((size_t)((tp * 2 + tt) * 32 + wid * 4) << 11) + i * 1024 + lane * 16);
    asm volatile("s_waitcnt vmcnt(16)" ::: "memory");
    f32x16 acc[2][2] = {};
    const LAS char* wfb = wl + (4 * hi + ((lane & 15) >> 2)) * 128 + ((lane >> 4) & 1) * 32 + (lane & 3) * 8;
#pragma unroll
    for (int i = 0; i < 8; ++i) {
        const int row0 = (i >> 1) * DH + (i & 1) * 16;
#pragma unroll
        for (int ct = 0; ct < 2; ++ct) {
            u32x2 a0 = tr_read(wfb + row0 * 128 + ct * 64), a1 = tr_read(wfb + (row0 + 8) * 128 + ct * 64);
            const u32x4 wf = {a0[0], a0[1], a1[0], a1[1]};
            acc[0][ct] = mfma32(wf, of[0][i], acc[0][ct]);
            acc[1][ct] = mfma32(wf, of[1][i], acc[1][ct]);
        }
    }
#pragma unroll
    for (int tt = 0; tt < 2; ++tt)
#pragma unroll
        for (int ct = 0; ct < 2; ++ct)
#pragma unroll
            for (int g = 0; g < 4; ++g)
                *(LAS f32x4*)(wl + ((tt * 2 + ct) * 4 + g) * 1024 + lane * 16) = (f32x4){acc[tt][ct][4 * g], acc[tt][ct][4 * g + 1], acc[tt][ct][4 * g + 2], acc[tt][ct][4 * g + 3]};
    __syncthreads();
    {
        const int blk = tid >> 5, l = tid & 31;
        f32x4 a = {}, b = {};
#pragma unroll
        for (int w = 0; w < 8; ++w) {
            a += *(const LAS f32x4*)(lds + w * 16384 + blk * 1024 + l * 16);
            b += *(const LAS f32x4*)(lds + w * 16384 + blk * 1024 + 512 + l * 16);
        }
        const int tt = blk >> 3, ct = (blk >> 2) & 1, g = blk & 3;
        const int token = tp * 64 + tt * 32 + l, col = cg * 64 + ct * 32 + g * 8;
        *(u32x4*)(out + (size_t)token * D_ + col) = (u32x4){cvtpk(a[0], a[1]), cvtpk(a[2], a[3]), cvtpk(b[0], b[1]), cvtpk(b[2], b[3])};
    }
}

extern "C" void kernel_launch(void* const* d_in, const int* in_sizes, int n_in, void* d_out, int out_size, void* d_ws, size_t ws_size, hipStream_t stream) {
    const uint16_t* x = (const uint16_t*)d_in[0];
    const uint16_t* wA = (const uint16_t*)d_in[1];
    const uint16_t* woA = (const uint16_t*)d_in[2];
    const uint16_t* wB = (const uint16_t*)d_in[4];
    const uint16_t* woB = (const uint16_t*)d_in[5];
    const uint16_t* gw = (const uint16_t*)d_in[6];
    (void)hipFuncSetAttribute((const void*)k_qkv, hipFuncAttributeMaxDynamicSharedMemorySize, P1_LDS);
    (void)hipFuncSetAttribute((const void*)k_attn, hipFuncAttributeMaxDynamicSharedMemorySize, P2_LDS);
    (void)hipFuncSetAttribute((const void*)k_out, hipFuncAttributeMaxDynamicSharedMemorySize, P3_LDS);
    k_qkv<<<256, 512, P1_LDS, stream>>>(x, wA, wB, gw, (char*)d_ws);
    k_attn<<<256, 512, P2_LDS, stream>>>((char*)d_ws);
    k_out<<<224, 512, P3_LDS, stream>>>(woA, woB, (const char*)d_ws, (uint16_t*)d_out);
}
```

```cpp
#include <hip/hip_runtime.h>
#include <hip/hip_bf16.h>
#include <stdint.h>

typedef __attribute__((ext_vector_type(8))) __bf16 bf16x8;
typedef __attribute__((ext_vector_type(16))) __bf16 bf16x16;
typedef __attribute__((ext_vector_type(2))) __bf16 bf16x2;
typedef __attribute__((ext_vector_type(16))) float f32x16;
typedef __attribute__((ext_vector_type(8))) float f32x8;
typedef __attribute__((ext_vector_type(4))) float f32x4;
typedef __attribute__((ext_vector_type(2))) float f32x2;
typedef __attribute__((ext_vector_type(16))) unsigned u32x16;
typedef __attribute__((ext_vector_type(8))) unsigned u32x8;
typedef __attribute__((ext_vector_type(4))) unsigned u32x4;
typedef __attribute__((ext_vector_type(2))) unsigned u32x2;
typedef short v4i16_t __attribute__((ext_vector_type(4)));
typedef unsigned long long u64;
#define LAS __attribute__((address_space(3)))
#define GAS __attribute__((address_space(1)))
#define RLX_AGENT __ATOMIC_RELAXED, __HIP_MEMORY_SCOPE_AGENT

#define SCH 1, 2, 3, 4, 10, 12, 14, 16
#define QPOS 24
#define KVE 0
#define SFL 3
#define SEQ 0
#define PFC 0
#define PFL 14
#define QFLAG 18
#define ROT 1
#define SLEEP 1
#define FS 128
#define MAPD 1
constexpr int D_ = 448, DH = 28, N3 = 1344;
constexpr float QSCALE = 0.18898223650461362f * 1.4426950408889634f;

constexpr size_t WS_EPOCH = 0;
constexpr size_t WS_FKV = 4096;
constexpr size_t WS_FO = 4096 + 256 * FS;
constexpr size_t WS_KV = 1u << 20;
constexpr size_t WS_O = WS_KV + (8u << 20);

constexpr int CH = 32768;
constexpr int L_S0 = 131072;
constexpr int L_WKV = 0;
constexpr int L_WQ = 0;
constexpr int L_XB = 57344, L_XC = 90112;
constexpr int L_CNT = 122880;
constexpr int L_WOA = 32768;
constexpr int L_WOB = 106496;
constexpr int LDS_BYTES = 163840;

__device__ __forceinline__ unsigned cvtpk(float lo, float hi) {
    return __builtin_bit_cast(unsigned, __builtin_convertvector((f32x2){lo, hi}, bf16x2));
}
__device__ __forceinline__ f32x16 mfma32(u32x4 a, u32x4 b, f32x16 c) {
    return __builtin_amdgcn_mfma_f32_32x32x16_bf16(__builtin_bit_cast(bf16x8, a), __builtin_bit_cast(bf16x8, b), c, 0, 0, 0);
}
__device__ __forceinline__ f32x16 smfma32(u32x4 a, f32x8 b, f32x16 c, int idx) {
    return __builtin_amdgcn_smfmac_f32_32x32x32_bf16(__builtin_bit_cast(bf16x8, a), __builtin_bit_cast(bf16x16, b), c, idx, 0, 0);
}
__device__ __forceinline__ u32x2 tr_read(const LAS char* p) {
    return __builtin_bit_cast(u32x2, __builtin_amdgcn_ds_read_tr16_b64_v4i16((LAS v4i16_t*)p));
}
__device__ __forceinline__ u32x4 tr_frag(const LAS char* p0, const LAS char* p1) {
    const u32x2 a = tr_read(p0), b = tr_read(p1); return (u32x4){a[0], a[1], b[0], b[1]};
}
__device__ __forceinline__ u32x4 lds_read16(const LAS char* p) { return *(const LAS u32x4*)p; }
__device__ __forceinline__ void lds_write16(LAS char* p, u32x4 v) { *(LAS u32x4*)p = v; }
__device__ __forceinline__ int swap23(int i) { return (i & 0x13) | ((i & 4) << 1) | ((i & 8) >> 1); }
__device__ __forceinline__ u32x4 pack_lo(const f32x16& a) { return (u32x4){cvtpk(a[0], a[1]), cvtpk(a[2], a[3]), cvtpk(a[4], a[5]), cvtpk(a[6], a[7])}; }
__device__ __forceinline__ u32x4 pack_hi(const f32x16& a) { return (u32x4){cvtpk(a[8], a[9]), cvtpk(a[10], a[11]), cvtpk(a[12], a[13]), cvtpk(a[14], a[15])}; }
__device__ __forceinline__ float xhalf_max(float v) {
    auto rr = __builtin_amdgcn_permlane32_swap(__float_as_uint(v), __float_as_uint(v), false, false);
    return fmaxf(__uint_as_float(rr[0]), __uint_as_float(rr[1]));
}
__device__ __forceinline__ float upper_half(float v) {
    auto rr = __builtin_amdgcn_permlane32_swap(__float_as_uint(v), __float_as_uint(v), false, false);
    return __uint_as_float(rr[1]);
}
__device__ __forceinline__ float max16(const f32x16& s) {
    float a = fmaxf(fmaxf(s[0], s[1]), s[2]);
#pragma unroll
    for (int i = 3; i < 15; i += 2) a = fmaxf(fmaxf(a, s[i]), s[i + 1]);
    return fmaxf(a, s[15]);
}
__device__ __forceinline__ void set_shift(u32x4& q1, float m, int hi) {
    const float nm = -m;
    const unsigned h0 = cvtpk(nm, 0.f) & 0xffffu; const float f0 = __uint_as_float(h0 << 16);
    const float r1 = nm - f0;
    const unsigned h1 = cvtpk(r1, 0.f) & 0xffffu; const float f1 = __uint_as_float(h1 << 16);
    const float r2 = r1 - f1;
    const unsigned h2 = cvtpk(r2, 0.f) & 0xffffu;
    q1[2] = hi ? (h0 | (h1 << 16)) : q1[2];
    q1[3] = hi ? h2 : q1[3];
}
__device__ __forceinline__ f32x8 lo8(const f32x16& v) { return __builtin_shufflevector(v, v, 0, 1, 2, 3, 4, 5, 6, 7); }
__device__ __forceinline__ f32x8 hi8(const f32x16& v) { return __builtin_shufflevector(v, v, 8, 9, 10, 11, 12, 13, 14, 15); }

__device__ __forceinline__ void wait_flags8(const GAS u64* f, u64 tag, int lane) {
#pragma unroll 1
    for (int spins = 0; spins < (1 << 20); ++spins) {
        const u64 v = __hip_atomic_load(f + (lane & 7), RLX_AGENT);
        if (__all(v == tag)) break;
        __builtin_amdgcn_s_sleep(SLEEP);
    }
}

#define DMA16(voff, srd, soff, ldsoff) asm volatile("s_mov_b32 m0, %3\n\ts_nop 0\n\tbuffer_load_dwordx4 %0, %1, %2 offen lds" :: "v"(voff), "s"(srd), "s"(soff), "s"(ldsoff) : "memory")
#define DMA16SC1(voff, srd, soff, ldsoff) asm volatile("s_mov_b32 m0, %3\n\ts_nop 0\n\tbuffer_load_dwordx4 %0, %1, %2 offen sc1 lds" :: "v"(voff), "s"(srd), "s"(soff), "s"(ldsoff) : "memory")
#define FLAGS(mask, voff, val, base) asm volatile("s_mov_b64 exec, %3\n\tglobal_store_dwordx2 %0, %1, %2 sc1\n\ts_mov_b64 exec, -1" :: "v"(voff), "v"(val), "s"(base), "n"(mask) : "memory")
#define BLD16(dst, voff, srd, soff) asm volatile("buffer_load_dwordx4 %0, %1, %2, %3 offen" : "=v"(dst) : "v"(voff), "s"(srd), "s"(soff) : "memory")

__device__ __forceinline__ u32x4 make_srd(const void* p, unsigned bytes) {
    const u64 a = (u64)p;
    return (u32x4){(unsigned)__builtin_amdgcn_readfirstlane((unsigned)a), (unsigned)__builtin_amdgcn_readfirstlane((unsigned)(a >> 32)), bytes, 0x00020000u};
}

#define ATTN_LOOP_ASM \
    "ds_read_b128 %[qa0], %[vxch] offset:57344\n" \
    "ds_read_b128 %[k00], %[vcur] offset:0\n" \
    "ds_read_b128 %[qa1], %[vxch] offset:58368\n" \
    "ds_read_b128 %[k01], %[vcur] offset:1024\n" \
    "ds_read_b128 %[k10], %[vcur] offset:8192\n" \
    "ds_read_b128 %[k11], %[vcur] offset:9216\n" \
    "ds_read_b128 %[v00], %[vcur] offset:2048\n" \
    "ds_read_b128 %[v01], %[vcur] offset:3072\n" \
    "s_mov_b32 %[sm0], m0\n" \
    "s_setprio 2\n" \
    "s_waitcnt lgkmcnt(6)\n" \
    "v_mfma_f32_32x32x16_bf16 v[128:143], %[k00], %[qa0], 0\n" \
    "s_waitcnt lgkmcnt(4)\n" \
    "v_mfma_f32_32x32x16_bf16 v[128:143], %[k01], %[qa1], v[128:143]\n" \
    "s_nop 7\n" \
    "s_nop 3\n" \
    "s_setprio 2\n" \
    "v_mfma_f32_32x32x16_bf16 v[144:159], %[k00], %[qb0], 0\n" \
    "v_exp_f32 v128, v128\n" \
    "v_exp_f32 v129, v129\n" \
    "v_exp_f32 v130, v130\n" \
    "v_exp_f32 v131, v131\n" \
    "v_mfma_f32_32x32x16_bf16 v[144:159], %[k01], %[qb1], v[144:159]\n" \
    "v_exp_f32 v132, v132\n" \
    "v_exp_f32 v133, v133\n" \
    "v_exp_f32 v134, v134\n" \
    "v_exp_f32 v135, v135\n" \
    "v_mfma_f32_32x32x16_bf16 %[oa], %[v10], %[v10], 0\n" \
    "global_load_dwordx2 %[fv], %[fvo], %[fptr] sc1\n" \
    "v_exp_f32 v136, v136\n" \
    "v_exp_f32 v137, v137\n" \
    "v_exp_f32 v138, v138\n" \
    "v_exp_f32 v139, v139\n" \
    "v_mfma_f32_32x32x16_bf16 %[ob], %[v10], %[v10], 0\n" \
    "v_exp_f32 v140, v140\n" \
    "v_exp_f32 v141, v141\n" \
    "v_exp_f32 v142, v142\n" \
    "v_exp_f32 v143, v143\n" \
    "s_waitcnt lgkmcnt(0)\n" \
    "v_mfma_f32_32x32x16_bf16 v[160:175], %[k10], %[qa0], 0\n" \
    "ds_read_b128 %[k00], %[vcur] offset:16384\n" \
    "v_exp_f32 v144, v144\n" \
    "v_exp_f32 v145, v145\n" \
    "v_exp_f32 v146, v146\n" \
    "v_exp_f32 v147, v147\n" \
    "v_mfma_f32_32x32x16_bf16 v[160:175], %[k11], %[qa1], v[160:175]\n" \
    "ds_read_b128 %[k01], %[vcur] offset:17408\n" \
    "v_exp_f32 v148, v148\n" \
    "v_exp_f32 v149, v149\n" \
    "v_exp_f32 v150, v150\n" \
    "v_exp_f32 v151, v151\n" \
    "v_smfmac_f32_32x32x32_bf16 %[oa], %[v00], v[128:135], %[idx]\n" \
    "ds_read_b128 %[v10], %[vcur] offset:10240\n" \
    "v_exp_f32 v152, v152\n" \
    "v_exp_f32 v153, v153\n" \
    "v_exp_f32 v154, v154\n" \
    "v_exp_f32 v155, v155\n" \
    "v_smfmac_f32_32x32x32_bf16 %[oa], %[v01], v[136:143], %[idx]\n" \
    "ds_read_b128 %[v11], %[vcur] offset:11264\n" \
    "v_exp_f32 v156, v156\n" \
    "v_exp_f32 v157, v157\n" \
    "v_exp_f32 v158, v158\n" \
    "v_exp_f32 v159, v159\n" \
    "s_setprio 1\n" \
    "v_mfma_f32_32x32x16_bf16 v[176:191], %[k10], %[qb0], 0\n" \
    "v_exp_f32 v160, v160\n" \
    "v_exp_f32 v161, v161\n" \
    "v_exp_f32 v162, v162\n" \
    "v_exp_f32 v163, v163\n" \
    "v_mfma_f32_32x32x16_bf16 v[176:191], %[k11], %[qb1], v[176:191]\n" \
    "v_exp_f32 v164, v164\n" \
    "v_exp_f32 v165, v165\n" \
    "v_exp_f32 v166, v166\n" \
    "v_exp_f32 v167, v167\n" \
    "v_smfmac_f32_32x32x32_bf16 %[ob], %[v00], v[144:151], %[idx]\n" \
    "4:\n" \
    "s_waitcnt vmcnt(0)\n" \
    "v_cmp_eq_u64 vcc, %[fv], %[tag]\n" \
    "s_and_b32 %[seen], vcc_lo, 0xff\n" \
    "s_bitcmp1_b32 %[seen], %[sbit]\n" \
    "s_cbranch_scc1 5f\n" \
    "s_sub_u32 %[spin], %[spin], 1\n" \
    "s_cselect_b32 %[seen], 0xff, %[seen]\n" \
    "s_cbranch_scc1 5f\n" \
    "s_sleep 1\n" \
    "global_load_dwordx2 %[fv], %[fvo], %[fptr] sc1\n" \
    "s_branch 4b\n" \
    "5:\n" \
    "buffer_load_dwordx4 v[112:115], %[dvoff], s[60:63], %[soff] offen sc1\n" \
    "s_add_u32 %[soff], %[soff], %[sstr]\n" \
    "buffer_load_dwordx4 v[116:119], %[dvoff], s[60:63], %[soff] offen sc1\n" \
    "s_add_u32 %[soff], %[soff], %[sstr]\n" \
    "v_exp_f32 v168, v168\n" \
    "v_exp_f32 v169, v169\n" \
    "v_exp_f32 v170, v170\n" \
    "v_exp_f32 v171, v171\n" \
    "v_smfmac_f32_32x32x32_bf16 %[ob], %[v01], v[152:159], %[idx]\n" \
    "buffer_load_dwordx4 v[120:123], %[dvoff], s[60:63], %[soff] offen sc1\n" \
    "s_add_u32 %[soff], %[soff], %[sstr]\n" \
    "buffer_load_dwordx4 v[124:127], %[dvoff], s[60:63], %[soff] offen sc1\n" \
    "s_lshr_b32 %[seq], %[seq], 4\n" \
    "s_and_b32 %[sbit], %[seq], 7\n" \
    "s_lshl_b32 %[soff], %[sbit], 15\n" \
    "s_add_u32 %[soff], %[soff], %[w4k]\n" \
    "v_exp_f32 v172, v172\n" \
    "v_exp_f32 v173, v173\n" \
    "v_exp_f32 v174, v174\n" \
    "v_exp_f32 v175, v175\n" \
    "s_waitcnt lgkmcnt(0)\n" \
    "v_mfma_f32_32x32x16_bf16 v[128:143], %[k00], %[qa0], 0\n" \
    "ds_read_b128 %[k10], %[vcur] offset:24576\n" \
    "v_exp_f32 v176, v176\n" \
    "v_exp_f32 v177, v177\n" \
    "v_exp_f32 v178, v178\n" \
    "v_exp_f32 v179, v179\n" \
    "v_mfma_f32_32x32x16_bf16 v[128:143], %[k01], %[qa1], v[128:143]\n" \
    "ds_read_b128 %[k11], %[vcur] offset:25600\n" \
    "v_exp_f32 v180, v180\n" \
    "v_exp_f32 v181, v181\n" \
    "v_exp_f32 v182, v182\n" \
    "v_exp_f32 v183, v183\n" \
    "v_smfmac_f32_32x32x32_bf16 %[oa], %[v10], v[160:167], %[idx]\n" \
    "ds_read_b128 %[v00], %[vcur] offset:18432\n" \
    "v_exp_f32 v184, v184\n" \
    "v_exp_f32 v185, v185\n" \
    "v_exp_f32 v186, v186\n" \
    "v_exp_f32 v187, v187\n" \
    "v_smfmac_f32_32x32x32_bf16 %[oa], %[v11], v[168:175], %[idx]\n" \
    "ds_read_b128 %[v01], %[vcur] offset:19456\n" \
    "v_exp_f32 v188, v188\n" \
    "v_exp_f32 v189, v189\n" \
    "v_exp_f32 v190, v190\n" \
    "v_exp_f32 v191, v191\n" \
    "s_setprio 0\n" \
    "v_mfma_f32_32x32x16_bf16 v[144:159], %[k00], %[qb0], 0\n" \
    "v_exp_f32 v128, v128\n" \
    "v_exp_f32 v129, v129\n" \
    "v_exp_f32 v130, v130\n" \
    "v_exp_f32 v131, v131\n" \
    "v_mfma_f32_32x32x16_bf16 v[144:159], %[k01], %[qb1], v[144:159]\n" \
    "v_exp_f32 v132, v132\n" \
    "v_exp_f32 v133, v133\n" \
    "v_exp_f32 v134, v134\n" \
    "v_exp_f32 v135, v135\n" \
    "v_smfmac_f32_32x32x32_bf16 %[ob], %[v10], v[176:183], %[idx]\n" \
    "v_exp_f32 v136, v136\n" \
    "v_exp_f32 v137, v137\n" \
    "v_exp_f32 v138, v138\n" \
    "v_exp_f32 v139, v139\n" \
    "v_smfmac_f32_32x32x32_bf16 %[ob], %[v11], v[184:191], %[idx]\n" \
    "v_exp_f32 v140, v140\n" \
    "v_exp_f32 v141, v141\n" \
    "v_exp_f32 v142, v142\n" \
    "v_exp_f32 v143, v143\n" \
    "s_waitcnt lgkmcnt(0)\n" \
    "v_mfma_f32_32x32x16_bf16 v[160:175], %[k10], %[qa0], 0\n" \
    "ds_read_b128 %[v10], %[vcur] offset:26624\n" \
    "v_exp_f32 v144, v144\n" \
    "v_exp_f32 v145, v145\n" \
    "v_exp_f32 v146, v146\n" \
    "v_exp_f32 v147, v147\n" \
    "v_mfma_f32_32x32x16_bf16 v[160:175], %[k11], %[qa1], v[160:175]\n" \
    "ds_read_b128 %[v11], %[vcur] offset:27648\n" \
    "v_exp_f32 v148, v148\n" \
    "v_exp_f32 v149, v149\n" \
    "v_exp_f32 v150, v150\n" \
    "v_exp_f32 v151, v151\n" \
    "v_smfmac_f32_32x32x32_bf16 %[oa], %[v00], v[128:135], %[idx]\n" \
    "s_waitcnt vmcnt(2)\n" \
    "ds_write_b128 %[vwr], v[112:115] offset:0\n" \
    "ds_write_b128 %[vwr], v[116:119] offset:1024\n" \
    "v_exp_f32 v152, v152\n" \
    "v_exp_f32 v153, v153\n" \
    "v_exp_f32 v154, v154\n" \
    "v_exp_f32 v155, v155\n" \
    "v_smfmac_f32_32x32x32_bf16 %[oa], %[v01], v[136:143], %[idx]\n" \
    "s_waitcnt vmcnt(0)\n" \
    "ds_write_b128 %[vwr], v[120:123] offset:2048\n" \
    "ds_write_b128 %[vwr], v[124:127] offset:3072\n" \
    "v_exp_f32 v156, v156\n" \
    "v_exp_f32 v157, v157\n" \
    "v_exp_f32 v158, v158\n" \
    "v_exp_f32 v159, v159\n" \
    "s_branch 13f\n" \
    ".p2align 6\n" \
    "1:\n" \
    "s_setprio 2\n" \
    "v_mfma_f32_32x32x16_bf16 v[144:159], %[k00], %[qb0], 0\n" \
    "v_exp_f32 v128, v128\n" \
    "v_exp_f32 v129, v129\n" \
    "v_exp_f32 v130, v130\n" \
    "v_exp_f32 v131, v131\n" \
    "v_mfma_f32_32x32x16_bf16 v[144:159], %[k01], %[qb1], v[144:159]\n" \
    "v_exp_f32 v132, v132\n" \
    "v_exp_f32 v133, v133\n" \
    "v_exp_f32 v134, v134\n" \
    "v_exp_f32 v135, v135\n" \
    "v_smfmac_f32_32x32x32_bf16 %[ob], %[v10], v[176:183], %[idx]\n" \
    "v_exp_f32 v136, v136\n" \
    "v_exp_f32 v137, v137\n" \
    "v_exp_f32 v138, v138\n" \
    "v_exp_f32 v139, v139\n" \
    "v_smfmac_f32_32x32x32_bf16 %[ob], %[v11], v[184:191], %[idx]\n" \
    "v_exp_f32 v140, v140\n" \
    "v_exp_f32 v141, v141\n" \
    "v_exp_f32 v142, v142\n" \
    "v_exp_f32 v143, v143\n" \
    "s_waitcnt lgkmcnt(0)\n" \
    "s_cmp_eq_u32 %[seen], 0xff\n" \
    "s_cbranch_scc1 5f\n" \
    "4:\n" \
    "s_waitcnt vmcnt(0)\n" \
    "v_cmp_eq_u64 vcc, %[fv], %[tag]\n" \
    "s_and_b32 %[seen], vcc_lo, 0xff\n" \
    "s_bitcmp1_b32 %[seen], %[sbit]\n" \
    "s_cbranch_scc1 5f\n" \
    "s_sub_u32 %[spin], %[spin], 1\n" \
    "s_cselect_b32 %[seen], 0xff, %[seen]\n" \
    "s_cbranch_scc1 5f\n" \
    "s_sleep 1\n" \
    "global_load_dwordx2 %[fv], %[fvo], %[fptr] sc1\n" \
    "s_branch 4b\n" \
    "5:\n" \
    "s_cmp_lg_u32 %[cnt], 1\n" \
    "s_cbranch_scc1 2f\n" \
    "s_mov_b64 s[60:61], s[64:65]\n" \
    "s_mov_b32 s62, s66\n" \
    "v_mov_b32 %[dvoff], %[wvoff]\n" \
    "s_mov_b32 %[sstr], 7168\n" \
    "s_mov_b32 %[soff], %[swb]\n" \
    "global_load_dwordx2 %[fv], %[fvo], %[fo] sc1\n" \
    "2:\n" \
    "v_mfma_f32_32x32x16_bf16 v[160:175], %[k10], %[qa0], 0\n" \
    "ds_read_b128 %[k00], %[vcur] offset:16384\n" \
    "buffer_load_dwordx4 v[112:115], %[dvoff], s[60:63], %[soff] offen sc1\n" \
    "s_add_u32 %[soff], %[soff], %[sstr]\n" \
    "v_exp_f32 v144, v144\n" \
    "v_exp_f32 v145, v145\n" \
    "v_exp_f32 v146, v146\n" \
    "v_exp_f32 v147, v147\n" \
    "v_mfma_f32_32x32x16_bf16 v[160:175], %[k11], %[qa1], v[160:175]\n" \
    "ds_read_b128 %[k01], %[vcur] offset:17408\n" \
    "buffer_load_dwordx4 v[116:119], %[dvoff], s[60:63], %[soff] offen sc1\n" \
    "s_add_u32 %[soff], %[soff], %[sstr]\n" \
    "v_exp_f32 v148, v148\n" \
    "v_exp_f32 v149, v149\n" \
    "v_exp_f32 v150, v150\n" \
    "v_exp_f32 v151, v151\n" \
    "v_smfmac_f32_32x32x32_bf16 %[oa], %[v00], v[128:135], %[idx]\n" \
    "ds_read_b128 %[v10], %[vcur] offset:10240\n" \
    "buffer_load_dwordx4 v[120:123], %[dvoff], s[60:63], %[soff] offen sc1\n" \
    "s_add_u32 %[soff], %[soff], %[sstr]\n" \
    "v_exp_f32 v152, v152\n" \
    "v_exp_f32 v153, v153\n" \
    "v_exp_f32 v154, v154\n" \
    "v_exp_f32 v155, v155\n" \
    "v_smfmac_f32_32x32x32_bf16 %[oa], %[v01], v[136:143], %[idx]\n" \
    "ds_read_b128 %[v11], %[vcur] offset:11264\n" \
    "buffer_load_dwordx4 v[124:127], %[dvoff], s[60:63], %[soff] offen sc1\n" \
    "s_lshr_b32 %[seq], %[seq], 4\n" \
    "s_and_b32 %[sbit], %[seq], 7\n" \
    "s_lshl_b32 %[soff], %[sbit], 15\n" \
    "s_add_u32 %[soff], %[soff], %[w4k]\n" \
    "v_exp_f32 v156, v156\n" \
    "v_exp_f32 v157, v157\n" \
    "v_exp_f32 v158, v158\n" \
    "v_exp_f32 v159, v159\n" \
    "s_setprio 1\n" \
    "v_mfma_f32_32x32x16_bf16 v[176:191], %[k10], %[qb0], 0\n" \
    "v_exp_f32 v160, v160\n" \
    "v_exp_f32 v161, v161\n" \
    "v_exp_f32 v162, v162\n" \
    "v_exp_f32 v163, v163\n" \
    "v_mfma_f32_32x32x16_bf16 v[176:191], %[k11], %[qb1], v[176:191]\n" \
    "v_exp_f32 v164, v164\n" \
    "v_exp_f32 v165, v165\n" \
    "v_exp_f32 v166, v166\n" \
    "v_exp_f32 v167, v167\n" \
    "v_smfmac_f32_32x32x32_bf16 %[ob], %[v00], v[144:151], %[idx]\n" \
    "v_exp_f32 v168, v168\n" \
    "v_exp_f32 v169, v169\n" \
    "v_exp_f32 v170, v170\n" \
    "v_exp_f32 v171, v171\n" \
    "v_smfmac_f32_32x32x32_bf16 %[ob], %[v01], v[152:159], %[idx]\n" \
    "v_exp_f32 v172, v172\n" \
    "v_exp_f32 v173, v173\n" \
    "v_exp_f32 v174, v174\n" \
    "v_exp_f32 v175, v175\n" \
    "s_waitcnt lgkmcnt(0)\n" \
    "v_mfma_f32_32x32x16_bf16 v[128:143], %[k00], %[qa0], 0\n" \
    "ds_read_b128 %[k10], %[vcur] offset:24576\n" \
    "v_exp_f32 v176, v176\n" \
    "v_exp_f32 v177, v177\n" \
    "v_exp_f32 v178, v178\n" \
    "v_exp_f32 v179, v179\n" \
    "v_mfma_f32_32x32x16_bf16 v[128:143], %[k01], %[qa1], v[128:143]\n" \
    "ds_read_b128 %[k11], %[vcur] offset:25600\n" \
    "v_exp_f32 v180, v180\n" \
    "v_exp_f32 v181, v181\n" \
    "v_exp_f32 v182, v182\n" \
    "v_exp_f32 v183, v183\n" \
    "v_smfmac_f32_32x32x32_bf16 %[oa], %[v10], v[160:167], %[idx]\n" \
    "ds_read_b128 %[v00], %[vcur] offset:18432\n" \
    "v_exp_f32 v184, v184\n" \
    "v_exp_f32 v185, v185\n" \
    "v_exp_f32 v186, v186\n" \
    "v_exp_f32 v187, v187\n" \
    "v_smfmac_f32_32x32x32_bf16 %[oa], %[v11], v[168:175], %[idx]\n" \
    "ds_read_b128 %[v01], %[vcur] offset:19456\n" \
    "v_exp_f32 v188, v188\n" \
    "v_exp_f32 v189, v189\n" \
    "v_exp_f32 v190, v190\n" \
    "v_exp_f32 v191, v191\n" \
    "s_setprio 0\n" \
    "s_waitcnt vmcnt(0)\n" \
    "v_mfma_f32_32x32x16_bf16 v[144:159], %[k00], %[qb0], 0\n" \
    "ds_write_b128 %[vwr], v[112:115] offset:0\n" \
    "v_exp_f32 v128, v128\n" \
    "v_exp_f32 v129, v129\n" \
    "v_exp_f32 v130, v130\n" \
    "v_exp_f32 v131, v131\n" \
    "v_mfma_f32_32x32x16_bf16 v[144:159], %[k01], %[qb1], v[144:159]\n" \
    "ds_write_b128 %[vwr], v[116:119] offset:1024\n" \
    "v_exp_f32 v132, v132\n" \
    "v_exp_f32 v133, v133\n" \
    "v_exp_f32 v134, v134\n" \
    "v_exp_f32 v135, v135\n" \
    "v_smfmac_f32_32x32x32_bf16 %[ob], %[v10], v[176:183], %[idx]\n" \
    "ds_write_b128 %[vwr], v[120:123] offset:2048\n" \
    "s_cmp_lg_u32 %[cnt], 4\n" \
    "s_cbranch_scc1 9f\n" \
    "v_mov_b32 v208, -1\n" \
    "v_mov_b32 v209, -1\n" \
    "v_mov_b32 v210, -1\n" \
    "v_mov_b32 v211, -1\n" \
    "buffer_store_dwordx4 v[208:211], %[l16], %[osrd], %[oso] offen sc1\n" \
    "buffer_store_dwordx4 v[208:211], %[l16], %[osrd], %[oso] offen offset:1024 sc1\n" \
    "9:\n" \
    "v_exp_f32 v136, v136\n" \
    "v_exp_f32 v137, v137\n" \
    "v_exp_f32 v138, v138\n" \
    "v_exp_f32 v139, v139\n" \
    "v_smfmac_f32_32x32x32_bf16 %[ob], %[v11], v[184:191], %[idx]\n" \
    "ds_write_b128 %[vwr], v[124:127] offset:3072\n" \
    "v_exp_f32 v140, v140\n" \
    "v_exp_f32 v141, v141\n" \
    "v_exp_f32 v142, v142\n" \
    "v_exp_f32 v143, v143\n" \
    "s_cmp_eq_u32 %[cnt], 1\n" \
    "s_cbranch_scc1 3f\n" \
    "s_waitcnt lgkmcnt(0)\n" \
    "v_mfma_f32_32x32x16_bf16 v[160:175], %[k10], %[qa0], 0\n" \
    "ds_read_b128 %[v10], %[vcur] offset:26624\n" \
    "v_exp_f32 v144, v144\n" \
    "v_exp_f32 v145, v145\n" \
    "v_exp_f32 v146, v146\n" \
    "v_exp_f32 v147, v147\n" \
    "v_mfma_f32_32x32x16_bf16 v[160:175], %[k11], %[qa1], v[160:175]\n" \
    "ds_read_b128 %[v11], %[vcur] offset:27648\n" \
    "v_exp_f32 v148, v148\n" \
    "v_exp_f32 v149, v149\n" \
    "v_exp_f32 v150, v150\n" \
    "v_exp_f32 v151, v151\n" \
    "v_smfmac_f32_32x32x32_bf16 %[oa], %[v00], v[128:135], %[idx]\n" \
    "v_exp_f32 v152, v152\n" \
    "v_exp_f32 v153, v153\n" \
    "v_exp_f32 v154, v154\n" \
    "v_exp_f32 v155, v155\n" \
    "v_smfmac_f32_32x32x32_bf16 %[oa], %[v01], v[136:143], %[idx]\n" \
    "v_exp_f32 v156, v156\n" \
    "v_exp_f32 v157, v157\n" \
    "v_exp_f32 v158, v158\n" \
    "v_exp_f32 v159, v159\n" \
    "13:\n" \
    "s_waitcnt lgkmcnt(0)\n" \
    "s_barrier\n" \
    "v_xor_b32 %[vcur], 0x20000, %[vcur]\n" \
    "v_xor_b32 %[vwr], 0x20000, %[vwr]\n" \
    "s_setprio 3\n" \
    "v_mfma_f32_32x32x16_bf16 v[176:191], %[k10], %[qb0], 0\n" \
    "ds_read_b128 %[k00], %[vcur] offset:0\n" \
    "v_exp_f32 v160, v160\n" \
    "v_exp_f32 v161, v161\n" \
    "v_exp_f32 v162, v162\n" \
    "v_exp_f32 v163, v163\n" \
    "v_mfma_f32_32x32x16_bf16 v[176:191], %[k11], %[qb1], v[176:191]\n" \
    "ds_read_b128 %[k01], %[vcur] offset:1024\n" \
    "v_exp_f32 v164, v164\n" \
    "v_exp_f32 v165, v165\n" \
    "v_exp_f32 v166, v166\n" \
    "v_exp_f32 v167, v167\n" \
    "v_smfmac_f32_32x32x32_bf16 %[ob], %[v00], v[144:151], %[idx]\n" \
    "s_cmp_lg_u32 %[cnt], %[sfl]\n" \
    "s_cbranch_scc1 8f\n" \
    "v_lshlrev_b32 v212, 5, %[l16]\n" \
    "s_mov_b64 exec, 0xfffffff\n" \
    "global_store_dwordx2 v212, %[tag], %[fw] sc1\n" \
    "s_mov_b64 exec, -1\n" \
    "8:\n" \
    "v_exp_f32 v168, v168\n" \
    "v_exp_f32 v169, v169\n" \
    "v_exp_f32 v170, v170\n" \
    "v_exp_f32 v171, v171\n" \
    "v_smfmac_f32_32x32x32_bf16 %[ob], %[v01], v[152:159], %[idx]\n" \
    "v_exp_f32 v172, v172\n" \
    "v_exp_f32 v173, v173\n" \
    "v_exp_f32 v174, v174\n" \
    "v_exp_f32 v175, v175\n" \
    "s_waitcnt lgkmcnt(0)\n" \
    "v_mfma_f32_32x32x16_bf16 v[128:143], %[k00], %[qa0], 0\n" \
    "ds_read_b128 %[k10], %[vcur] offset:8192\n" \
    "s_mov_b32 m0, %[swad]\n" \
    "s_nop 0\n" \
    "buffer_load_dwordx4 %[wvoff], %[wasrd], %[swao] offen lds\n" \
    "v_exp_f32 v176, v176\n" \
    "v_exp_f32 v177, v177\n" \
    "v_exp_f32 v178, v178\n" \
    "v_exp_f32 v179, v179\n" \
    "v_mfma_f32_32x32x16_bf16 v[128:143], %[k01], %[qa1], v[128:143]\n" \
    "ds_read_b128 %[k11], %[vcur] offset:9216\n" \
    "v_exp_f32 v180, v180\n" \
    "v_exp_f32 v181, v181\n" \
    "v_exp_f32 v182, v182\n" \
    "v_exp_f32 v183, v183\n" \
    "v_smfmac_f32_32x32x32_bf16 %[oa], %[v10], v[160:167], %[idx]\n" \
    "ds_read_b128 %[v00], %[vcur] offset:2048\n" \
    "s_add_u32 m0, %[swad], 0x12000\n" \
    "s_cmp_lt_u32 %[cnt], 6\n" \
    "s_cbranch_scc1 6f\n" \
    "buffer_load_dwordx4 %[wvoff], %[wbsrd], %[swao] offen lds\n" \
    "6:\n" \
    "s_add_u32 %[swad], %[swad], 0x2000\n" \
    "s_add_u32 %[swao], %[swao], 57344\n" \
    "v_exp_f32 v184, v184\n" \
    "v_exp_f32 v185, v185\n" \
    "v_exp_f32 v186, v186\n" \
    "v_exp_f32 v187, v187\n" \
    "v_smfmac_f32_32x32x32_bf16 %[oa], %[v11], v[168:175], %[idx]\n" \
    "ds_read_b128 %[v01], %[vcur] offset:3072\n" \
    "s_cmp_eq_u32 %[seen], 0xff\n" \
    "s_cbranch_scc1 7f\n" \
    "global_load_dwordx2 %[fv], %[fvo], %[fptr] sc1\n" \
    "7:\n" \
    "v_exp_f32 v188, v188\n" \
    "v_exp_f32 v189, v189\n" \
    "v_exp_f32 v190, v190\n" \
    "v_exp_f32 v191, v191\n" \
    "s_sub_u32 %[cnt], %[cnt], 1\n" \
    "s_branch 1b\n" \
    "10:\n" \
    "s_sub_u32 %[spin], %[spin], 1\n" \
    "s_cbranch_scc1 11f\n" \
    "s_sleep 1\n" \
    "global_load_dwordx2 %[fv], %[fvo], %[fo] sc1\n" \
    "3:\n" \
    "s_waitcnt vmcnt(0)\n" \
    "v_cmp_ne_u64 vcc, %[fv], %[tag]\n" \
    "s_cbranch_vccnz 10b\n" \
    "11:\n" \
    "s_waitcnt lgkmcnt(0)\n" \
    "v_mfma_f32_32x32x16_bf16 v[160:175], %[k10], %[qa0], 0\n" \
    "ds_read_b128 %[v10], %[vcur] offset:26624\n" \
    "v_exp_f32 v144, v144\n" \
    "v_exp_f32 v145, v145\n" \
    "v_exp_f32 v146, v146\n" \
    "v_exp_f32 v147, v147\n" \
    "v_mfma_f32_32x32x16_bf16 v[160:175], %[k11], %[qa1], v[160:175]\n" \
    "ds_read_b128 %[v11], %[vcur] offset:27648\n" \
    "v_exp_f32 v148, v148\n" \
    "v_exp_f32 v149, v149\n" \
    "v_exp_f32 v150, v150\n" \
    "v_exp_f32 v151, v151\n" \
    "v_smfmac_f32_32x32x32_bf16 %[oa], %[v00], v[128:135], %[idx]\n" \
    "v_exp_f32 v152, v152\n" \
    "v_exp_f32 v153, v153\n" \
    "v_exp_f32 v154, v154\n" \
    "v_exp_f32 v155, v155\n" \
    "v_smfmac_f32_32x32x32_bf16 %[oa], %[v01], v[136:143], %[idx]\n" \
    "v_exp_f32 v156, v156\n" \
    "v_exp_f32 v157, v157\n" \
    "v_exp_f32 v158, v158\n" \
    "v_exp_f32 v159, v159\n" \
    "s_waitcnt lgkmcnt(0)\n" \
    "s_barrier\n" \
    "s_setprio 3\n" \
    "v_mfma_f32_32x32x16_bf16 v[176:191], %[k10], %[qb0], 0\n" \
    "v_exp_f32 v160, v160\n" \
    "v_exp_f32 v161, v161\n" \
    "v_exp_f32 v162, v162\n" \
    "v_exp_f32 v163, v163\n" \
    "v_mfma_f32_32x32x16_bf16 v[176:191], %[k11], %[qb1], v[176:191]\n" \
    "v_exp_f32 v164, v164\n" \
    "v_exp_f32 v165, v165\n" \
    "v_exp_f32 v166, v166\n" \
    "v_exp_f32 v167, v167\n" \
    "v_smfmac_f32_32x32x32_bf16 %[ob], %[v00], v[144:151], %[idx]\n" \
    "v_exp_f32 v168, v168\n" \
    "v_exp_f32 v169, v169\n" \
    "v_exp_f32 v170, v170\n" \
    "v_exp_f32 v171, v171\n" \
    "v_smfmac_f32_32x32x32_bf16 %[ob], %[v01], v[152:159], %[idx]\n" \
    "v_exp_f32 v172, v172\n" \
    "v_exp_f32 v173, v173\n" \
    "v_exp_f32 v174, v174\n" \
    "v_exp_f32 v175, v175\n" \
    "v_smfmac_f32_32x32x32_bf16 %[oa], %[v10], v[160:167], %[idx]\n" \
    "v_exp_f32 v176, v176\n" \
    "v_exp_f32 v177, v177\n" \
    "v_exp_f32 v178, v178\n" \
    "v_exp_f32 v179, v179\n" \
    "v_smfmac_f32_32x32x32_bf16 %[oa], %[v11], v[168:175], %[idx]\n" \
    "v_exp_f32 v180, v180\n" \
    "v_exp_f32 v181, v181\n" \
    "v_exp_f32 v182, v182\n" \
    "v_exp_f32 v183, v183\n" \
    "v_exp_f32 v184, v184\n" \
    "v_exp_f32 v185, v185\n" \
    "v_exp_f32 v186, v186\n" \
    "v_exp_f32 v187, v187\n" \
    "v_smfmac_f32_32x32x32_bf16 %[ob], %[v10], v[176:183], %[idx]\n" \
    "v_exp_f32 v188, v188\n" \
    "v_exp_f32 v189, v189\n" \
    "v_exp_f32 v190, v190\n" \
    "v_exp_f32 v191, v191\n" \
    "ds_write_b128 %[vxch], v[192:195] offset:0\n" \
    "ds_write_b128 %[vxch], v[196:199] offset:1024\n" \
    "v_smfmac_f32_32x32x32_bf16 %[ob], %[v11], v[184:191], %[idx]\n" \
    "ds_write_b128 %[vxch], v[200:203] offset:2048\n" \
    "ds_write_b128 %[vxch], v[204:207] offset:3072\n" \
    "s_setprio 0\n" \
    "s_mov_b32 m0, %[sm0]\n" \
    "s_nop 7\n" \
    "s_waitcnt lgkmcnt(0)\n" \
    "s_barrier\n" \
    "ds_read_b128 v[204:207], %[vown] offset:3072\n" \
    "ds_read_b128 v[192:195], %[vown] offset:0\n" \
    "ds_read_b128 v[196:199], %[vown] offset:1024\n" \
    "ds_read_b128 v[200:203], %[vown] offset:2048\n"
#define ATTN_LOOP_CLOB "v128", "v129", "v130", "v131", "v132", "v133", "v134", "v135", "v136", "v137", "v138", "v139", "v140", "v141", "v142", "v143", "v144", "v145", "v146", "v147", "v148", "v149", "v150", "v151", "v152", "v153", "v154", "v155", "v156", "v157", "v158", "v159", "v160", "v161", "v162", "v163", "v164", "v165", "v166", "v167", "v168", "v169", "v170", "v171", "v172", "v173", "v174", "v175", "v176", "v177", "v178", "v179", "v180", "v181", "v182", "v183", "v184", "v185", "v186", "v187", "v188", "v189", "v190", "v191", "s68", "s69", "vcc"
#define P3_ASM \
    "v_mov_b32 v232, -1\n" \
    "v_mov_b32 v236, -1\n" \
    "v_mov_b32 v240, -1\n" \
    "v_mov_b32 v244, -1\n" \
    "v_mov_b32 v248, -1\n" \
    "v_mov_b32 v252, -1\n" \
    "v_mov_b32 v112, -1\n" \
    "v_mov_b32 v116, -1\n" \
    "v_mov_b32 v120, -1\n" \
    "v_mov_b32 v124, -1\n" \
    "s_mov_b32 %[wd], 0\n" \
    "ds_read_b64_tr_b16 v[128:129], %[w0] offset:0\n" \
    "ds_read_b64_tr_b16 v[130:131], %[w0] offset:1024\n" \
    "ds_read_b64_tr_b16 v[132:133], %[w1] offset:0\n" \
    "ds_read_b64_tr_b16 v[134:135], %[w1] offset:1024\n" \
    "ds_read_b64_tr_b16 v[136:137], %[w0] offset:2048\n" \
    "ds_read_b64_tr_b16 v[138:139], %[w0] offset:3072\n" \
    "ds_read_b64_tr_b16 v[140:141], %[w1] offset:2048\n" \
    "ds_read_b64_tr_b16 v[142:143], %[w1] offset:3072\n" \
    "ds_read_b64_tr_b16 v[144:145], %[w0] offset:3584\n" \
    "ds_read_b64_tr_b16 v[146:147], %[w0] offset:4608\n" \
    "ds_read_b64_tr_b16 v[148:149], %[w1] offset:3584\n" \
    "ds_read_b64_tr_b16 v[150:151], %[w1] offset:4608\n" \
    "ds_read_b64_tr_b16 v[152:153], %[w0] offset:5632\n" \
    "ds_read_b64_tr_b16 v[154:155], %[w0] offset:6656\n" \
    "ds_read_b64_tr_b16 v[156:157], %[w1] offset:5632\n" \
    "ds_read_b64_tr_b16 v[158:159], %[w1] offset:6656\n" \
    "s_barrier\n" \
    "buffer_load_dwordx4 v[208:211], %[l16], %[osrd], %[so0] offen offset:0 sc1\n" \
    "buffer_load_dwordx4 v[212:215], %[l16], %[osrd], %[so0] offen offset:1024 sc1\n" \
    "buffer_load_dwordx4 v[216:219], %[l16], %[osrd], %[so0] offen offset:2048 sc1\n" \
    "buffer_load_dwordx4 v[220:223], %[l16], %[osrd], %[so0] offen offset:3072 sc1\n" \
    "buffer_load_dwordx4 v[224:227], %[l16], %[osrd], %[so1] offen offset:0 sc1\n" \
    "buffer_load_dwordx4 v[228:231], %[l16], %[osrd], %[so1] offen offset:1024 sc1\n" \
    "ds_read_b64_tr_b16 v[160:161], %[w0] offset:7168\n" \
    "ds_read_b64_tr_b16 v[162:163], %[w0] offset:8192\n" \
    "ds_read_b64_tr_b16 v[164:165], %[w1] offset:7168\n" \
    "ds_read_b64_tr_b16 v[166:167], %[w1] offset:8192\n" \
    "ds_read_b64_tr_b16 v[168:169], %[w0] offset:9216\n" \
    "ds_read_b64_tr_b16 v[170:171], %[w0] offset:10240\n" \
    "ds_read_b64_tr_b16 v[172:173], %[w1] offset:9216\n" \
    "ds_read_b64_tr_b16 v[174:175], %[w1] offset:10240\n" \
    "ds_read_b64_tr_b16 v[176:177], %[w0] offset:10752\n" \
    "ds_read_b64_tr_b16 v[178:179], %[w0] offset:11776\n" \
    "ds_read_b64_tr_b16 v[180:181], %[w1] offset:10752\n" \
    "ds_read_b64_tr_b16 v[182:183], %[w1] offset:11776\n" \
    "ds_read_b64_tr_b16 v[184:185], %[w0] offset:12800\n" \
    "ds_read_b64_tr_b16 v[186:187], %[w0] offset:13824\n" \
    "ds_read_b64_tr_b16 v[188:189], %[w1] offset:12800\n" \
    "ds_read_b64_tr_b16 v[190:191], %[w1] offset:13824\n" \
    "s_waitcnt vmcnt(5)\n" \
    "v_max3_u32 %[t], v208, v209, v210\n" \
    "v_max_u32 %[t], %[t], v211\n" \
    "s_waitcnt vmcnt(4) lgkmcnt(12)\n" \
    "v_max3_u32 %[t], v212, v213, %[t]\n" \
    "v_max3_u32 %[t], v214, v215, %[t]\n" \
    "v_cmp_eq_u32 vcc, -1, %[t]\n" \
    "s_cbranch_vccnz 40f\n" \
    "v_mfma_f32_32x32x16_bf16 %[a1], v[132:135], v[208:211], 0\n" \
    "buffer_load_dwordx4 v[232:235], %[l16], %[osrd], %[so1] offen offset:2048 sc1\n" \
    "ds_read_b64_tr_b16 v[192:193], %[w0] offset:14336\n" \
    "ds_read_b64_tr_b16 v[194:195], %[w0] offset:15360\n" \
    "ds_read_b64_tr_b16 v[196:197], %[w1] offset:14336\n" \
    "ds_read_b64_tr_b16 v[198:199], %[w1] offset:15360\n" \
    "v_mfma_f32_32x32x16_bf16 %[a0], v[128:131], v[208:211], 0\n" \
    "buffer_load_dwordx4 v[236:239], %[l16], %[osrd], %[so1] offen offset:3072 sc1\n" \
    "ds_read_b64_tr_b16 v[200:201], %[w0] offset:16384\n" \
    "ds_read_b64_tr_b16 v[202:203], %[w0] offset:17408\n" \
    "ds_read_b64_tr_b16 v[204:205], %[w1] offset:16384\n" \
    "ds_read_b64_tr_b16 v[206:207], %[w1] offset:17408\n" \
    "v_mfma_f32_32x32x16_bf16 %[a1], v[140:143], v[212:215], %[a1]\n" \
    "s_waitcnt vmcnt(5)\n" \
    "v_max3_u32 %[t], v216, v217, v218\n" \
    "v_max_u32 %[t], %[t], v219\n" \
    "v_mfma_f32_32x32x16_bf16 %[a0], v[136:139], v[212:215], %[a0]\n" \
    "s_waitcnt vmcnt(4) lgkmcnt(12)\n" \
    "v_max3_u32 %[t], v220, v221, %[t]\n" \
    "v_max3_u32 %[t], v222, v223, %[t]\n" \
    "v_cmp_eq_u32 vcc, -1, %[t]\n" \
    "s_cbranch_vccnz 41f\n" \
    "v_mfma_f32_32x32x16_bf16 %[a1], v[148:151], v[216:219], %[a1]\n" \
    "buffer_load_dwordx4 v[240:243], %[l16], %[osrd], %[so2] offen offset:0 sc1\n" \
    "ds_read_b64_tr_b16 v[64:65], %[w0] offset:17920\n" \
    "ds_read_b64_tr_b16 v[66:67], %[w0] offset:18944\n" \
    "ds_read_b64_tr_b16 v[68:69], %[w1] offset:17920\n" \
    "ds_read_b64_tr_b16 v[70:71], %[w1] offset:18944\n" \
    "v_mfma_f32_32x32x16_bf16 %[a0], v[144:147], v[216:219], %[a0]\n" \
    "buffer_load_dwordx4 v[244:247], %[l16], %[osrd], %[so2] offen offset:1024 sc1\n" \
    "ds_read_b64_tr_b16 v[72:73], %[w0] offset:19968\n" \
    "ds_read_b64_tr_b16 v[74:75], %[w0] offset:20992\n" \
    "ds_read_b64_tr_b16 v[76:77], %[w1] offset:19968\n" \
    "ds_read_b64_tr_b16 v[78:79], %[w1] offset:20992\n" \
    "v_mfma_f32_32x32x16_bf16 %[a1], v[156:159], v[220:223], %[a1]\n" \
    "s_waitcnt vmcnt(5)\n" \
    "v_max3_u32 %[t], v224, v225, v226\n" \
    "v_max_u32 %[t], %[t], v227\n" \
    "v_mfma_f32_32x32x16_bf16 %[a0], v[152:155], v[220:223], %[a0]\n" \
    "s_waitcnt vmcnt(4) lgkmcnt(12)\n" \
    "v_max3_u32 %[t], v228, v229, %[t]\n" \
    "v_max3_u32 %[t], v230, v231, %[t]\n" \
    "v_cmp_eq_u32 vcc, -1, %[t]\n" \
    "s_cbranch_vccnz 42f\n" \
    "v_mfma_f32_32x32x16_bf16 %[a1], v[164:167], v[224:227], %[a1]\n" \
    "buffer_load_dwordx4 v[248:251], %[l16], %[osrd], %[so2] offen offset:2048 sc1\n" \
    "ds_read_b64_tr_b16 v[80:81], %[w0] offset:21504\n" \
    "ds_read_b64_tr_b16 v[82:83], %[w0] offset:22528\n" \
    "ds_read_b64_tr_b16 v[84:85], %[w1] offset:21504\n" \
    "ds_read_b64_tr_b16 v[86:87], %[w1] offset:22528\n" \
    "v_mfma_f32_32x32x16_bf16 %[a0], v[160:163], v[224:227], %[a0]\n" \
    "buffer_load_dwordx4 v[252:255], %[l16], %[osrd], %[so2] offen offset:3072 sc1\n" \
    "ds_read_b64_tr_b16 v[88:89], %[w0] offset:23552\n" \
    "ds_read_b64_tr_b16 v[90:91], %[w0] offset:24576\n" \
    "ds_read_b64_tr_b16 v[92:93], %[w1] offset:23552\n" \
    "ds_read_b64_tr_b16 v[94:95], %[w1] offset:24576\n" \
    "v_mfma_f32_32x32x16_bf16 %[a1], v[172:175], v[228:231], %[a1]\n" \
    "s_waitcnt vmcnt(5)\n" \
    "v_max3_u32 %[t], v232, v233, v234\n" \
    "v_max_u32 %[t], %[t], v235\n" \
    "v_mfma_f32_32x32x16_bf16 %[a0], v[168:171], v[228:231], %[a0]\n" \
    "s_waitcnt vmcnt(4) lgkmcnt(12)\n" \
    "v_max3_u32 %[t], v236, v237, %[t]\n" \
    "v_max3_u32 %[t], v238, v239, %[t]\n" \
    "v_cmp_eq_u32 vcc, -1, %[t]\n" \
    "s_cbranch_vccnz 43f\n" \
    "v_mfma_f32_32x32x16_bf16 %[a1], v[180:183], v[232:235], %[a1]\n" \
    "buffer_load_dwordx4 v[112:115], %[l16], %[osrd], %[so3] offen offset:0 sc1\n" \
    "ds_read_b64_tr_b16 v[96:97], %[w0] offset:25088\n" \
    "ds_read_b64_tr_b16 v[98:99], %[w0] offset:26112\n" \
    "ds_read_b64_tr_b16 v[100:101], %[w1] offset:25088\n" \
    "ds_read_b64_tr_b16 v[102:103], %[w1] offset:26112\n" \
    "v_mfma_f32_32x32x16_bf16 %[a0], v[176:179], v[232:235], %[a0]\n" \
    "buffer_load_dwordx4 v[116:119], %[l16], %[osrd], %[so3] offen offset:1024 sc1\n" \
    "ds_read_b64_tr_b16 v[104:105], %[w0] offset:27136\n" \
    "ds_read_b64_tr_b16 v[106:107], %[x0] offset:27136\n" \
    "ds_read_b64_tr_b16 v[108:109], %[w1] offset:27136\n" \
    "ds_read_b64_tr_b16 v[110:111], %[x1] offset:27136\n" \
    "v_mfma_f32_32x32x16_bf16 %[a1], v[188:191], v[236:239], %[a1]\n" \
    "s_waitcnt vmcnt(5)\n" \
    "v_max3_u32 %[t], v240, v241, v242\n" \
    "v_max_u32 %[t], %[t], v243\n" \
    "v_mfma_f32_32x32x16_bf16 %[a0], v[184:187], v[236:239], %[a0]\n" \
    "s_waitcnt vmcnt(4) lgkmcnt(12)\n" \
    "v_max3_u32 %[t], v244, v245, %[t]\n" \
    "v_max3_u32 %[t], v246, v247, %[t]\n" \
    "v_cmp_eq_u32 vcc, -1, %[t]\n" \
    "s_cbranch_vccnz 44f\n" \
    "v_mfma_f32_32x32x16_bf16 %[a1], v[196:199], v[240:243], %[a1]\n" \
    "buffer_load_dwordx4 v[120:123], %[l16], %[osrd], %[so3] offen offset:2048 sc1\n" \
    "v_mfma_f32_32x32x16_bf16 %[a0], v[192:195], v[240:243], %[a0]\n" \
    "buffer_load_dwordx4 v[124:127], %[l16], %[osrd], %[so3] offen offset:3072 sc1\n" \
    "v_mfma_f32_32x32x16_bf16 %[a1], v[204:207], v[244:247], %[a1]\n" \
    "s_waitcnt vmcnt(5)\n" \
    "v_max3_u32 %[t], v248, v249, v250\n" \
    "v_max_u32 %[t], %[t], v251\n" \
    "v_mfma_f32_32x32x16_bf16 %[a0], v[200:203], v[244:247], %[a0]\n" \
    "s_waitcnt vmcnt(4) lgkmcnt(12)\n" \
    "v_max3_u32 %[t], v252, v253, %[t]\n" \
    "v_max3_u32 %[t], v254, v255, %[t]\n" \
    "v_cmp_eq_u32 vcc, -1, %[t]\n" \
    "s_cbranch_vccnz 45f\n" \
    "25:\n" \
    "v_mfma_f32_32x32x16_bf16 %[a1], v[68:71], v[248:251], %[a1]\n" \
    "v_mfma_f32_32x32x16_bf16 %[a0], v[64:67], v[248:251], %[a0]\n" \
    "v_mfma_f32_32x32x16_bf16 %[a1], v[76:79], v[252:255], %[a1]\n" \
    "s_waitcnt vmcnt(3)\n" \
    "v_max3_u32 %[t], v112, v113, v114\n" \
    "v_max_u32 %[t], %[t], v115\n" \
    "v_mfma_f32_32x32x16_bf16 %[a0], v[72:75], v[252:255], %[a0]\n" \
    "s_waitcnt vmcnt(2) lgkmcnt(8)\n" \
    "v_max3_u32 %[t], v116, v117, %[t]\n" \
    "v_max3_u32 %[t], v118, v119, %[t]\n" \
    "v_cmp_eq_u32 vcc, -1, %[t]\n" \
    "s_cbranch_vccnz 46f\n" \
    "26:\n" \
    "v_mfma_f32_32x32x16_bf16 %[a1], v[84:87], v[112:115], %[a1]\n" \
    "v_mfma_f32_32x32x16_bf16 %[a0], v[80:83], v[112:115], %[a0]\n" \
    "v_mfma_f32_32x32x16_bf16 %[a1], v[92:95], v[116:119], %[a1]\n" \
    "s_waitcnt vmcnt(1)\n" \
    "v_max3_u32 %[t], v120, v121, v122\n" \
    "v_max_u32 %[t], %[t], v123\n" \
    "v_mfma_f32_32x32x16_bf16 %[a0], v[88:91], v[116:119], %[a0]\n" \
    "s_waitcnt vmcnt(0) lgkmcnt(0)\n" \
    "v_max3_u32 %[t], v124, v125, %[t]\n" \
    "v_max3_u32 %[t], v126, v127, %[t]\n" \
    "v_cmp_eq_u32 vcc, -1, %[t]\n" \
    "s_cbranch_vccnz 47f\n" \
    "27:\n" \
    "v_mfma_f32_32x32x16_bf16 %[a1], v[100:103], v[120:123], %[a1]\n" \
    "v_mfma_f32_32x32x16_bf16 %[a0], v[96:99], v[120:123], %[a0]\n" \
    "v_mfma_f32_32x32x16_bf16 %[a1], v[108:111], v[124:127], %[a1]\n" \
    "v_mfma_f32_32x32x16_bf16 %[a0], v[104:107], v[124:127], %[a0]\n" \
    "s_branch 99f\n" \
    "40:\n" \
    "s_call_b64 %[ret], 98f\n" \
    "s_call_b64 %[ret], 100f\n" \
    "s_waitcnt vmcnt(14)\n" \
    "v_max3_u32 %[t], v208, v209, v210\n" \
    "v_max_u32 %[t], %[t], v211\n" \
    "v_max3_u32 %[t], v212, v213, %[t]\n" \
    "v_max3_u32 %[t], v214, v215, %[t]\n" \
    "v_cmp_eq_u32 vcc, -1, %[t]\n" \
    "s_cbranch_vccnz 40b\n" \
    "s_branch 20f\n" \
    "41:\n" \
    "s_call_b64 %[ret], 98f\n" \
    "s_call_b64 %[ret], 102f\n" \
    "s_waitcnt vmcnt(12)\n" \
    "v_max3_u32 %[t], v216, v217, v218\n" \
    "v_max_u32 %[t], %[t], v219\n" \
    "v_max3_u32 %[t], v220, v221, %[t]\n" \
    "v_max3_u32 %[t], v222, v223, %[t]\n" \
    "v_cmp_eq_u32 vcc, -1, %[t]\n" \
    "s_cbranch_vccnz 41b\n" \
    "s_branch 21f\n" \
    "42:\n" \
    "s_call_b64 %[ret], 98f\n" \
    "s_call_b64 %[ret], 104f\n" \
    "s_waitcnt vmcnt(10)\n" \
    "v_max3_u32 %[t], v224, v225, v226\n" \
    "v_max_u32 %[t], %[t], v227\n" \
    "v_max3_u32 %[t], v228, v229, %[t]\n" \
    "v_max3_u32 %[t], v230, v231, %[t]\n" \
    "v_cmp_eq_u32 vcc, -1, %[t]\n" \
    "s_cbranch_vccnz 42b\n" \
    "s_branch 22f\n" \
    "43:\n" \
    "s_call_b64 %[ret], 98f\n" \
    "s_call_b64 %[ret], 106f\n" \
    "s_waitcnt vmcnt(8)\n" \
    "v_max3_u32 %[t], v232, v233, v234\n" \
    "v_max_u32 %[t], %[t], v235\n" \
    "v_max3_u32 %[t], v236, v237, %[t]\n" \
    "v_max3_u32 %[t], v238, v239, %[t]\n" \
    "v_cmp_eq_u32 vcc, -1, %[t]\n" \
    "s_cbranch_vccnz 43b\n" \
    "s_branch 23f\n" \
    "44:\n" \
    "s_call_b64 %[ret], 98f\n" \
    "s_call_b64 %[ret], 108f\n" \
    "s_waitcnt vmcnt(6)\n" \
    "v_max3_u32 %[t], v240, v241, v242\n" \
    "v_max_u32 %[t], %[t], v243\n" \
    "v_max3_u32 %[t], v244, v245, %[t]\n" \
    "v_max3_u32 %[t], v246, v247, %[t]\n" \
    "v_cmp_eq_u32 vcc, -1, %[t]\n" \
    "s_cbranch_vccnz 44b\n" \
    "s_branch 24f\n" \
    "45:\n" \
    "s_call_b64 %[ret], 98f\n" \
    "s_call_b64 %[ret], 110f\n" \
    "s_waitcnt vmcnt(4)\n" \
    "v_max3_u32 %[t], v248, v249, v250\n" \
    "v_max_u32 %[t], %[t], v251\n" \
    "v_max3_u32 %[t], v252, v253, %[t]\n" \
    "v_max3_u32 %[t], v254, v255, %[t]\n" \
    "v_cmp_eq_u32 vcc, -1, %[t]\n" \
    "s_cbranch_vccnz 45b\n" \
    "s_branch 25b\n" \
    "46:\n" \
    "s_call_b64 %[ret], 98f\n" \
    "s_call_b64 %[ret], 112f\n" \
    "s_waitcnt vmcnt(2)\n" \
    "v_max3_u32 %[t], v112, v113, v114\n" \
    "v_max_u32 %[t], %[t], v115\n" \
    "v_max3_u32 %[t], v116, v117, %[t]\n" \
    "v_max3_u32 %[t], v118, v119, %[t]\n" \
    "v_cmp_eq_u32 vcc, -1, %[t]\n" \
    "s_cbranch_vccnz 46b\n" \
    "s_branch 26b\n" \
    "47:\n" \
    "s_call_b64 %[ret], 98f\n" \
    "s_call_b64 %[ret], 114f\n" \
    "s_waitcnt vmcnt(0)\n" \
    "v_max3_u32 %[t], v120, v121, v122\n" \
    "v_max_u32 %[t], %[t], v123\n" \
    "v_max3_u32 %[t], v124, v125, %[t]\n" \
    "v_max3_u32 %[t], v126, v127, %[t]\n" \
    "v_cmp_eq_u32 vcc, -1, %[t]\n" \
    "s_cbranch_vccnz 47b\n" \
    "s_branch 27b\n" \
    "20:\n" \
    "v_mfma_f32_32x32x16_bf16 %[a1], v[132:135], v[208:211], 0\n" \
    "v_mfma_f32_32x32x16_bf16 %[a0], v[128:131], v[208:211], 0\n" \
    "v_mfma_f32_32x32x16_bf16 %[a1], v[140:143], v[212:215], %[a1]\n" \
    "s_waitcnt vmcnt(13)\n" \
    "v_max3_u32 %[t], v216, v217, v218\n" \
    "v_max_u32 %[t], %[t], v219\n" \
    "v_mfma_f32_32x32x16_bf16 %[a0], v[136:139], v[212:215], %[a0]\n" \
    "s_waitcnt vmcnt(12)\n" \
    "v_max3_u32 %[t], v220, v221, %[t]\n" \
    "v_max3_u32 %[t], v222, v223, %[t]\n" \
    "v_cmp_eq_u32 vcc, -1, %[t]\n" \
    "s_cbranch_vccnz 41b\n" \
    "21:\n" \
    "v_mfma_f32_32x32x16_bf16 %[a1], v[148:151], v[216:219], %[a1]\n" \
    "v_mfma_f32_32x32x16_bf16 %[a0], v[144:147], v[216:219], %[a0]\n" \
    "v_mfma_f32_32x32x16_bf16 %[a1], v[156:159], v[220:223], %[a1]\n" \
    "s_waitcnt vmcnt(11)\n" \
    "v_max3_u32 %[t], v224, v225, v226\n" \
    "v_max_u32 %[t], %[t], v227\n" \
    "v_mfma_f32_32x32x16_bf16 %[a0], v[152:155], v[220:223], %[a0]\n" \
    "s_waitcnt vmcnt(10)\n" \
    "v_max3_u32 %[t], v228, v229, %[t]\n" \
    "v_max3_u32 %[t], v230, v231, %[t]\n" \
    "v_cmp_eq_u32 vcc, -1, %[t]\n" \
    "s_cbranch_vccnz 42b\n" \
    "22:\n" \
    "v_mfma_f32_32x32x16_bf16 %[a1], v[164:167], v[224:227], %[a1]\n" \
    "v_mfma_f32_32x32x16_bf16 %[a0], v[160:163], v[224:227], %[a0]\n" \
    "v_mfma_f32_32x32x16_bf16 %[a1], v[172:175], v[228:231], %[a1]\n" \
    "s_waitcnt vmcnt(9)\n" \
    "v_max3_u32 %[t], v232, v233, v234\n" \
    "v_max_u32 %[t], %[t], v235\n" \
    "v_mfma_f32_32x32x16_bf16 %[a0], v[168:171], v[228:231], %[a0]\n" \
    "s_waitcnt vmcnt(8)\n" \
    "v_max3_u32 %[t], v236, v237, %[t]\n" \
    "v_max3_u32 %[t], v238, v239, %[t]\n" \
    "v_cmp_eq_u32 vcc, -1, %[t]\n" \
    "s_cbranch_vccnz 43b\n" \
    "23:\n" \
    "v_mfma_f32_32x32x16_bf16 %[a1], v[180:183], v[232:235], %[a1]\n" \
    "v_mfma_f32_32x32x16_bf16 %[a0], v[176:179], v[232:235], %[a0]\n" \
    "v_mfma_f32_32x32x16_bf16 %[a1], v[188:191], v[236:239], %[a1]\n" \
    "s_waitcnt vmcnt(7)\n" \
    "v_max3_u32 %[t], v240, v241, v242\n" \
    "v_max_u32 %[t], %[t], v243\n" \
    "v_mfma_f32_32x32x16_bf16 %[a0], v[184:187], v[236:239], %[a0]\n" \
    "s_waitcnt vmcnt(6)\n" \
    "v_max3_u32 %[t], v244, v245, %[t]\n" \
    "v_max3_u32 %[t], v246, v247, %[t]\n" \
    "v_cmp_eq_u32 vcc, -1, %[t]\n" \
    "s_cbranch_vccnz 44b\n" \
    "24:\n" \
    "v_mfma_f32_32x32x16_bf16 %[a1], v[196:199], v[240:243], %[a1]\n" \
    "v_mfma_f32_32x32x16_bf16 %[a0], v[192:195], v[240:243], %[a0]\n" \
    "v_mfma_f32_32x32x16_bf16 %[a1], v[204:207], v[244:247], %[a1]\n" \
    "s_waitcnt vmcnt(5)\n" \
    "v_max3_u32 %[t], v248, v249, v250\n" \
    "v_max_u32 %[t], %[t], v251\n" \
    "v_mfma_f32_32x32x16_bf16 %[a0], v[200:203], v[244:247], %[a0]\n" \
    "s_waitcnt vmcnt(4)\n" \
    "v_max3_u32 %[t], v252, v253, %[t]\n" \
    "v_max3_u32 %[t], v254, v255, %[t]\n" \
    "v_cmp_eq_u32 vcc, -1, %[t]\n" \
    "s_cbranch_vccnz 45b\n" \
    "s_branch 25b\n" \
    "98:\n" \
    "s_cmp_lg_u32 %[wd], 0\n" \
    "s_cbranch_scc1 97f\n" \
    "ds_read_b64_tr_b16 v[192:193], %[w0] offset:14336\n" \
    "ds_read_b64_tr_b16 v[194:195], %[w0] offset:15360\n" \
    "ds_read_b64_tr_b16 v[196:197], %[w1] offset:14336\n" \
    "ds_read_b64_tr_b16 v[198:199], %[w1] offset:15360\n" \
    "ds_read_b64_tr_b16 v[200:201], %[w0] offset:16384\n" \
    "ds_read_b64_tr_b16 v[202:203], %[w0] offset:17408\n" \
    "ds_read_b64_tr_b16 v[204:205], %[w1] offset:16384\n" \
    "ds_read_b64_tr_b16 v[206:207], %[w1] offset:17408\n" \
    "ds_read_b64_tr_b16 v[64:65], %[w0] offset:17920\n" \
    "ds_read_b64_tr_b16 v[66:67], %[w0] offset:18944\n" \
    "ds_read_b64_tr_b16 v[68:69], %[w1] offset:17920\n" \
    "ds_read_b64_tr_b16 v[70:71], %[w1] offset:18944\n" \
    "ds_read_b64_tr_b16 v[72:73], %[w0] offset:19968\n" \
    "ds_read_b64_tr_b16 v[74:75], %[w0] offset:20992\n" \
    "ds_read_b64_tr_b16 v[76:77], %[w1] offset:19968\n" \
    "ds_read_b64_tr_b16 v[78:79], %[w1] offset:20992\n" \
    "ds_read_b64_tr_b16 v[80:81], %[w0] offset:21504\n" \
    "ds_read_b64_tr_b16 v[82:83], %[w0] offset:22528\n" \
    "ds_read_b64_tr_b16 v[84:85], %[w1] offset:21504\n" \
    "ds_read_b64_tr_b16 v[86:87], %[w1] offset:22528\n" \
    "ds_read_b64_tr_b16 v[88:89], %[w0] offset:23552\n" \
    "ds_read_b64_tr_b16 v[90:91], %[w0] offset:24576\n" \
    "ds_read_b64_tr_b16 v[92:93], %[w1] offset:23552\n" \
    "ds_read_b64_tr_b16 v[94:95], %[w1] offset:24576\n" \
    "ds_read_b64_tr_b16 v[96:97], %[w0] offset:25088\n" \
    "ds_read_b64_tr_b16 v[98:99], %[w0] offset:26112\n" \
    "ds_read_b64_tr_b16 v[100:101], %[w1] offset:25088\n" \
    "ds_read_b64_tr_b16 v[102:103], %[w1] offset:26112\n" \
    "ds_read_b64_tr_b16 v[104:105], %[w0] offset:27136\n" \
    "ds_read_b64_tr_b16 v[106:107], %[x0] offset:27136\n" \
    "ds_read_b64_tr_b16 v[108:109], %[w1] offset:27136\n" \
    "ds_read_b64_tr_b16 v[110:111], %[x1] offset:27136\n" \
    "s_mov_b32 %[wd], 1\n" \
    "97:\n" \
    "s_sub_u32 %[spin], %[spin], 1\n" \
    "s_cbranch_scc1 96f\n" \
    "s_waitcnt vmcnt(0)\n" \
    "s_setpc_b64 %[ret]\n" \
    "96:\n" \
    "s_endpgm\n" \
    "100:\n" \
    "v_max3_u32 %[t], v208, v209, v210\n" \
    "v_max_u32 %[t], %[t], v211\n" \
    "v_cmp_eq_u32 vcc, -1, %[t]\n" \
    "s_or_b64 exec, vcc, 1\n" \
    "buffer_load_dwordx4 v[208:211], %[l16], %[osrd], %[so0] offen offset:0 sc1\n" \
    "s_mov_b64 exec, -1\n" \
    "v_max3_u32 %[t], v212, v213, v214\n" \
    "v_max_u32 %[t], %[t], v215\n" \
    "v_cmp_eq_u32 vcc, -1, %[t]\n" \
    "s_or_b64 exec, vcc, 1\n" \
    "buffer_load_dwordx4 v[212:215], %[l16], %[osrd], %[so0] offen offset:1024 sc1\n" \
    "s_mov_b64 exec, -1\n" \
    "102:\n" \
    "v_max3_u32 %[t], v216, v217, v218\n" \
    "v_max_u32 %[t], %[t], v219\n" \
    "v_cmp_eq_u32 vcc, -1, %[t]\n" \
    "s_or_b64 exec, vcc, 1\n" \
    "buffer_load_dwordx4 v[216:219], %[l16], %[osrd], %[so0] offen offset:2048 sc1\n" \
    "s_mov_b64 exec, -1\n" \
    "v_max3_u32 %[t], v220, v221, v222\n" \
    "v_max_u32 %[t], %[t], v223\n" \
    "v_cmp_eq_u32 vcc, -1, %[t]\n" \
    "s_or_b64 exec, vcc, 1\n" \
    "buffer_load_dwordx4 v[220:223], %[l16], %[osrd], %[so0] offen offset:3072 sc1\n" \
    "s_mov_b64 exec, -1\n" \
    "104:\n" \
    "v_max3_u32 %[t], v224, v225, v226\n" \
    "v_max_u32 %[t], %[t], v227\n" \
    "v_cmp_eq_u32 vcc, -1, %[t]\n" \
    "s_or_b64 exec, vcc, 1\n" \
    "buffer_load_dwordx4 v[224:227], %[l16], %[osrd], %[so1] offen offset:0 sc1\n" \
    "s_mov_b64 exec, -1\n" \
    "v_max3_u32 %[t], v228, v229, v230\n" \
    "v_max_u32 %[t], %[t], v231\n" \
    "v_cmp_eq_u32 vcc, -1, %[t]\n" \
    "s_or_b64 exec, vcc, 1\n" \
    "buffer_load_dwordx4 v[228:231], %[l16], %[osrd], %[so1] offen offset:1024 sc1\n" \
    "s_mov_b64 exec, -1\n" \
    "106:\n" \
    "v_max3_u32 %[t], v232, v233, v234\n" \
    "v_max_u32 %[t], %[t], v235\n" \
    "v_cmp_eq_u32 vcc, -1, %[t]\n" \
    "s_or_b64 exec, vcc, 1\n" \
    "buffer_load_dwordx4 v[232:235], %[l16], %[osrd], %[so1] offen offset:2048 sc1\n" \
    "s_mov_b64 exec, -1\n" \
    "v_max3_u32 %[t], v236, v237, v238\n" \
    "v_max_u32 %[t], %[t], v239\n" \
    "v_cmp_eq_u32 vcc, -1, %[t]\n" \
    "s_or_b64 exec, vcc, 1\n" \
    "buffer_load_dwordx4 v[236:239], %[l16], %[osrd], %[so1] offen offset:3072 sc1\n" \
    "s_mov_b64 exec, -1\n" \
    "108:\n" \
    "v_max3_u32 %[t], v240, v241, v242\n" \
    "v_max_u32 %[t], %[t], v243\n" \
    "v_cmp_eq_u32 vcc, -1, %[t]\n" \
    "s_or_b64 exec, vcc, 1\n" \
    "buffer_load_dwordx4 v[240:243], %[l16], %[osrd], %[so2] offen offset:0 sc1\n" \
    "s_mov_b64 exec, -1\n" \
    "v_max3_u32 %[t], v244, v245, v246\n" \
    "v_max_u32 %[t], %[t], v247\n" \
    "v_cmp_eq_u32 vcc, -1, %[t]\n" \
    "s_or_b64 exec, vcc, 1\n" \
    "buffer_load_dwordx4 v[244:247], %[l16], %[osrd], %[so2] offen offset:1024 sc1\n" \
    "s_mov_b64 exec, -1\n" \
    "110:\n" \
    "v_max3_u32 %[t], v248, v249, v250\n" \
    "v_max_u32 %[t], %[t], v251\n" \
    "v_cmp_eq_u32 vcc, -1, %[t]\n" \
    "s_or_b64 exec, vcc, 1\n" \
    "buffer_load_dwordx4 v[248:251], %[l16], %[osrd], %[so2] offen offset:2048 sc1\n" \
    "s_mov_b64 exec, -1\n" \
    "v_max3_u32 %[t], v252, v253, v254\n" \
    "v_max_u32 %[t], %[t], v255\n" \
    "v_cmp_eq_u32 vcc, -1, %[t]\n" \
    "s_or_b64 exec, vcc, 1\n" \
    "buffer_load_dwordx4 v[252:255], %[l16], %[osrd], %[so2] offen offset:3072 sc1\n" \
    "s_mov_b64 exec, -1\n" \
    "112:\n" \
    "v_max3_u32 %[t], v112, v113, v114\n" \
    "v_max_u32 %[t], %[t], v115\n" \
    "v_cmp_eq_u32 vcc, -1, %[t]\n" \
    "s_or_b64 exec, vcc, 1\n" \
    "buffer_load_dwordx4 v[112:115], %[l16], %[osrd], %[so3] offen offset:0 sc1\n" \
    "s_mov_b64 exec, -1\n" \
    "v_max3_u32 %[t], v116, v117, v118\n" \
    "v_max_u32 %[t], %[t], v119\n" \
    "v_cmp_eq_u32 vcc, -1, %[t]\n" \
    "s_or_b64 exec, vcc, 1\n" \
    "buffer_load_dwordx4 v[116:119], %[l16], %[osrd], %[so3] offen offset:1024 sc1\n" \
    "s_mov_b64 exec, -1\n" \
    "114:\n" \
    "v_max3_u32 %[t], v120, v121, v122\n" \
    "v_max_u32 %[t], %[t], v123\n" \
    "v_cmp_eq_u32 vcc, -1, %[t]\n" \
    "s_or_b64 exec, vcc, 1\n" \
    "buffer_load_dwordx4 v[120:123], %[l16], %[osrd], %[so3] offen offset:2048 sc1\n" \
    "s_mov_b64 exec, -1\n" \
    "v_max3_u32 %[t], v124, v125, v126\n" \
    "v_max_u32 %[t], %[t], v127\n" \
    "v_cmp_eq_u32 vcc, -1, %[t]\n" \
    "s_or_b64 exec, vcc, 1\n" \
    "buffer_load_dwordx4 v[124:127], %[l16], %[osrd], %[so3] offen offset:3072 sc1\n" \
    "s_mov_b64 exec, -1\n" \
    "s_waitcnt lgkmcnt(0)\n" \
    "s_setpc_b64 %[ret]\n" \
    "99:\n" \
    "s_nop 7\n" \
    "s_nop 2\n"
#define P3_CLOB "v64", "v65", "v66", "v67", "v68", "v69", "v70", "v71", "v72", "v73", "v74", "v75", "v76", "v77", "v78", "v79", "v80", "v81", "v82", "v83", "v84", "v85", "v86", "v87", "v88", "v89", "v90", "v91", "v92", "v93", "v94", "v95", "v96", "v97", "v98", "v99", "v100", "v101", "v102", "v103", "v104", "v105", "v106", "v107", "v108", "v109", "v110", "v111", "v128", "v129", "v130", "v131", "v132", "v133", "v134", "v135", "v136", "v137", "v138", "v139", "v140", "v141", "v142", "v143", "v144", "v145", "v146", "v147", "v148", "v149", "v150", "v151", "v152", "v153", "v154", "v155", "v156", "v157", "v158", "v159", "v160", "v161", "v162", "v163", "v164", "v165", "v166", "v167", "v168", "v169", "v170", "v171", "v172", "v173", "v174", "v175", "v176", "v177", "v178", "v179", "v180", "v181", "v182", "v183", "v184", "v185", "v186", "v187", "v188", "v189", "v190", "v191", "v192", "v193", "v194", "v195", "v196", "v197", "v198", "v199", "v200", "v201", "v202", "v203", "v204", "v205", "v206", "v207", "vcc"

__global__ __launch_bounds__(512, 2) void k_fused(const uint16_t* __restrict__ x_, const uint16_t* __restrict__ wA_, const uint16_t* __restrict__ wB_,
                                                 const uint16_t* __restrict__ woA_, const uint16_t* __restrict__ woB_, const uint16_t* __restrict__ gw_,
                                                 uint16_t* __restrict__ out_, char* __restrict__ ws_) {
    extern __shared__ __attribute__((aligned(16))) char smem[];
    LAS char* lds = (LAS char*)smem;
    u32x8 ka, kb;
    asm volatile("s_load_dwordx8 %0, %2, 0x0\n\ts_load_dwordx8 %1, %2, 0x20" : "=&s"(ka), "=&s"(kb) : "s"(__builtin_amdgcn_kernarg_segment_ptr()));
    const int tid = threadIdx.x, lane = tid & 63, wid = __builtin_amdgcn_readfirstlane(tid >> 6);
    unsigned junk;
    auto code_ahead = [&]() {
        u64 pc; asm volatile("s_getpc_b64 %0" : "=s"(pc));
        asm volatile("global_load_dword %0, %1, %2" : "=v"(junk) : "v"(tid * (PFL * 2)), "s"(pc) : "memory");
    };
    if (PFC == 0) code_ahead();
    const int xcd = blockIdx.x & 7, idx = blockIdx.x >> 3;
    const int hb = (xcd >> 1) * 8 + (idx >> 2), r = (xcd & 1) * 4 + (idx & 3);
    const int rho = ROT == 0 ? 0 : ROT == 2 ? (((idx >> 2) ^ idx) & 1) * 3 : ((idx >> 2) + 2 * (idx & 3)) % 7;
    const int h = hb & 15;
    const int r32 = lane & 31, hi = lane >> 5, half = wid >> 2;
    const int T = r * 8 + wid;

    u32x4 xr[28];
    const int cho = (lane & 3) * 16 - ((lane & 3) == 3 ? 8 : 0);
    const int tro = ((lane >> 4) & 1) * 32 + (((lane & 3) ^ ((lane >> 4) & (lane >> 1) & 1)) * 8);
    unsigned wv = (unsigned)(((lane >> 2) & 7) * (N3 * 2) + (1 + hi) * (D_ * 2) + h * (DH * 2) + cho);
    unsigned xv0 = (unsigned)((lane >> 3) * (D_ * 2) + ((lane & 7) ^ (lane >> 4)) * 16), xv1 = xv0 ^ 64;
    unsigned xb = (unsigned)((r * 256 + wid * 32) * (D_ * 2)), wo0 = (unsigned)((rho * 64 + wid * 8) * (N3 * 2)), xo0 = xb + rho * 128, w1k = wid * 1024, zero = 0;
    asm volatile("s_waitcnt lgkmcnt(0)" : "+s"(ka), "+s"(kb), "+v"(wv), "+v"(xv0), "+v"(xv1), "+v"(zero), "+s"(xb), "+s"(wo0), "+s"(xo0), "+s"(w1k));
    auto arg = [](const u32x8& k, int i) { return (char*)(((u64)k[2 * i + 1] << 32) | k[2 * i]); };
    const char *x = arg(ka, 0), *w = (hb < 16) ? arg(ka, 1) : arg(ka, 2), *woA = arg(ka, 3), *woB = arg(kb, 0), *gw = arg(kb, 1);
    char *out = arg(kb, 2), *ws = arg(kb, 3);
    const u32x4 wsrd = make_srd(w, D_ * N3 * 2), xsrd = make_srd(x, 2048 * D_ * 2);
    u64 tag;
    asm volatile("global_load_dwordx2 %0, %1, %2 sc1" : "=v"(tag) : "v"(zero), "s"(ws + WS_EPOCH) : "memory");
    auto xbuf = [](int j) { return j % 3 == 0 ? L_S0 : j % 3 == 1 ? L_XB : L_XC; };
    auto issue = [&](int j) {
        int s = j + rho; if (s >= 7) s -= 7;
        DMA16(wv, wsrd, j ? (unsigned)((s * 64 + wid * 8) * (N3 * 2)) : wo0, L_WKV + j * 8192 + w1k);
#pragma unroll
        for (int q = 0; q < 4; ++q) DMA16((q & 1) ? xv1 : xv0, xsrd, (j ? xb + s * 128 : xo0) + q * 8 * D_ * 2, xbuf(j) + w1k * 4 + q * 1024);
    };
    unsigned gwv;
    const int gsh = 4 - (((lane >> 4) & (lane >> 5)) & 1);
    const int girow = (lane >> 4) * 128 + (wid << gsh) + (lane & ((1 << gsh) - 1));
    auto issue_q = [&]() {
        const unsigned qv = (unsigned)((lane >> 2) * (N3 * 2) + h * (DH * 2) + cho);
#pragma unroll
        for (int i = 0; i < 3; ++i) {
            const int p = wid + 8 * i;
            int s = (p >> 2) + rho; if (s >= 7) s -= 7;
            DMA16(qv, wsrd, (unsigned)((s * 64 + (p & 3) * 16) * (N3 * 2)), (unsigned)(L_WQ + p * 1024));
        }
        int s = 6 + rho; if (s >= 7) s -= 7;
        asm volatile("s_mov_b32 m0, %3\n\ts_mov_b32 exec_hi, 0\n\tbuffer_load_dwordx4 %0, %1, %2 offen lds\n\ts_mov_b32 exec_hi, -1"
                     :: "v"(qv), "s"(wsrd), "s"((unsigned)((s * 64 + wid * 8) * (N3 * 2))), "s"((unsigned)(L_WQ + 24576 + wid * 512)) : "memory");
        int sg = (girow >> 6) + rho; if (sg >= 7) sg -= 7;
        asm volatile("global_load_ushort %0, %1, %2" : "=v"(gwv) : "v"((unsigned)((sg * 64 + (girow & 63)) * 2)), "s"(gw) : "memory");
    };
    if (PFC == 1) code_ahead();
    issue(0);
    if (PFC == 2) code_ahead();
    issue(1);
    if (PFC == 3) code_ahead();
    if (tid == 0) *(LAS unsigned*)(lds + L_CNT) = 0u;
    const LAS char* xs = lds + wid * 4096 + r32 * 128;
    const LAS char* wfb = lds + L_WKV + hi * 1024 + ((lane & 15) >> 2) * 64 + tro;
    const LAS char* qfb = lds + L_WQ + (8 * hi + ((lane & 15) >> 2)) * 64 + tro;
    f32x16 aK = {}, aV = {}, aQ = {};
    u32x4 fw[8];
    auto read_x = [&](int j) {
#pragma unroll
        for (int ks = 0; ks < 4; ++ks) xr[4 * j + ks] = lds_read16(xs + xbuf(j) + (((2 * ks + hi) ^ ((r32 >> 1) & 7)) << 4));
    };
    auto read_w = [&](int j, int ks) { const LAS char* p = wfb + (j * 8 + ks * 2) * 1024; fw[2 * ks] = tr_frag(p, p + 256); fw[2 * ks + 1] = tr_frag(p + 512, p + 768); };
    auto read_q = [&](int n) { fw[n & 7] = tr_frag(qfb + n * 1024, qfb + n * 1024 + 256); };
    auto mm = [&](int j, int ks) { aK = mfma32(fw[2 * ks], xr[4 * j + ks], aK); aV = mfma32(xr[4 * j + ks], fw[2 * ks + 1], aV); };
#define SB_ __builtin_amdgcn_sched_barrier(0)
    const int cg = (idx < 28) ? idx % 7 : 0, tp = xcd * 4 + min(idx, 27) / 7, tt = wid & 1, kq = wid >> 1;
    u32x4 osrd = make_srd(ws + WS_O, 4u << 20), kvsrd = make_srd(ws + WS_KV + ((size_t)hb << 18), 1u << 18);
    u32x4 wasrd = make_srd(woA, D_ * D_ * 2), wbsrd = make_srd(woB, D_ * D_ * 2);
    unsigned so0 = (unsigned)(((tp * 2 + tt) * 32 + kq * 8) * 2048), so1 = so0 + 4096, so2 = so0 + 8192, so3 = so0 + 12288;
    unsigned oso = (unsigned)((T * 32 + hb) * 2048), kso = (unsigned)(T * 4096), sfl = wid == 0 ? SFL : 0;
    unsigned seq = 0;
#pragma unroll
    for (int i = 1; i <= 8; ++i) seq |= (unsigned)(SEQ ? ((r ^ i) & 4) | ((r + i) & 3) : (r + i) & 7) << (4 * i - 4);
    unsigned cnt = 8, sbit = seq & 7, w4k = wid * 4096, soff = sbit * CH + w4k, sstr = 1024, seen = 0, spin = 1u << 20;
    unsigned swad = L_WOA + wid * 1024, swao = (unsigned)(wid * 8 * D_ * 2 + cg * 128), swb = (unsigned)((192 + wid * 32) * D_ * 2 + cg * 128);
    char *fkw = ws + WS_FKV + hb * 8 * FS + r * 8, *fkr = ws + WS_FKV + (hb * 8 + r) * FS;
    char *fow = ws + WS_FO + (r * 28 * 4 + (hb >> 3)) * FS + (hb & 7) * 8, *fr = ws + WS_FO + ((xcd * 28 + min(idx, 27)) * 4 + kq) * FS;
    char* outp = (char*)out + ((tp * 64 + tt * 32) * D_ + cg * 64 + kq * 16) * 2;
    unsigned wbase = ((kq & 2) ? L_WOB : L_WOA) + (kq & 1) * (224 * 128);
    asm volatile("" : "+s"(osrd), "+s"(kvsrd), "+s"(wasrd), "+s"(wbsrd), "+s"(so0), "+s"(so1), "+s"(so2), "+s"(so3), "+s"(oso), "+s"(kso), "+s"(sfl),
                      "+s"(cnt), "+s"(soff), "+s"(sstr), "+s"(seen), "+s"(sbit), "+s"(spin), "+s"(swad), "+s"(swao), "+s"(swb), "+s"(fkw), "+s"(fkr), "+s"(fow), "+s"(fr), "+s"(outp), "+s"(wbase), "+s"(seq), "+s"(w4k));
    asm volatile("s_waitcnt vmcnt(%0)\n\ts_barrier" :: "n"(PFC == 2 || PFC == 3 ? 6 : 5) : "memory");
    read_x(0); read_w(0, 0); read_w(0, 1); read_w(0, 2); read_w(0, 3);
    SB_; issue(2); SB_;
    if (PFC == 4) code_ahead();
#pragma unroll
    for (int j = 0; j < 6; ++j) {
        mm(j, 0); mm(j, 1); SB_;
        asm volatile("s_waitcnt vmcnt(%0)\n\ts_barrier" :: "n"(PFC == 4 && j == 0 ? 6 : 5) : "memory");
        read_x(j + 1); read_w(j + 1, 0); read_w(j + 1, 1);
        SB_; mm(j, 2); mm(j, 3); SB_;
        read_w(j + 1, 2); read_w(j + 1, 3);
        SB_;
        if (j < 4) issue(j + 3);
        if (j == 4) issue_q();
        SB_;
    }
    u32x4 k0, k1, v0, v1;
    const int p16 = (swap23(r32) + 32 * hi) * 16;
    constexpr int at[8] = {SCH};
#define ST_(v, a, o) asm volatile("buffer_store_dwordx4 %0, %1, %2, %3 offen offset:" #o " sc1\n\ts_nop 1" :: "v"(v), "v"(a), "s"(kvsrd), "s"(kso) : "memory")
    auto pub = [&](int n) {
        const unsigned f = (r32 == DH) ? 0x3f803f80u : 0u;
        LAS char* own = lds + L_S0 + wid * 4096;
        if (n == at[0]) { k0 = pack_lo(aK); ST_(k0, p16, 0); }
        if (n == at[1]) { k1 = pack_hi(aK); if (hi) { k1[2] = 0x3f803f80u; k1[3] = 0x00003f80u; } ST_(k1, p16, 1024); }
        if (n == at[2]) { v0 = pack_lo(aV); if (r32 >= DH) v0 = (u32x4){f, f, f, f}; ST_(v0, lane * 16, 2048); }
        if (n == at[3]) { v1 = pack_hi(aV); if (r32 >= DH) v1 = (u32x4){f, f, f, f}; ST_(v1, lane * 16, 3072); }
        if (n == at[4]) lds_write16(own + p16, k0);
        if (n == at[5]) lds_write16(own + 1024 + p16, k1);
        if (n == at[6]) lds_write16(own + 2048 + lane * 16, v0);
        if (n == at[7]) lds_write16(own + 3072 + lane * 16, v1);
        SB_;
    };
    mm(6, 0); mm(6, 1); SB_;
    if (KVE) { mm(6, 2); mm(6, 3); SB_; pub(-1); }
    asm volatile("s_waitcnt vmcnt(%3)" : "+v"(gwv), "+v"(tag), "+v"(junk) : "n"((at[0] < 0) + (at[1] < 0) + (at[2] < 0) + (at[3] < 0)) : "memory");
    *(LAS uint16_t*)(lds + L_WQ + girow * 64 + 48) = (uint16_t)gwv;
    asm volatile("s_waitcnt lgkmcnt(0)\n\ts_barrier" ::: "memory");
    read_q(0); read_q(1); read_q(2); read_q(3);
    SB_;
    if (!KVE) { mm(6, 2); mm(6, 3); SB_; }
    read_q(4); read_q(5); read_q(6); read_q(7);
    SB_;
    auto mq = [&](int n0, int n1) {
#pragma unroll
        for (int n = n0; n < n1; ++n) { aQ = mfma32(fw[n & 7], xr[n], aQ); SB_; if (n + 8 < 28) read_q(n + 8); SB_; }
    };
#pragma unroll
    for (int n = 0; n < QFLAG; ++n) { pub(n); mq(n, n + 1); }
    tag += 1;
    const __amdgpu_buffer_rsrc_t kvr = __builtin_amdgcn_make_buffer_rsrc(ws + WS_KV + ((size_t)hb << 18), 0, 1 << 18, 0x00020000);
    unsigned fso = lane * FS;
    auto signal = [&]() {
    asm volatile("s_waitcnt vmcnt(0)" ::: "memory");
    {
        unsigned n;
        asm volatile("s_mov_b64 exec, 1\n\tds_add_rtn_u32 %0, %1, %2\n\ts_mov_b64 exec, -1\n\ts_waitcnt lgkmcnt(0)" : "=v"(n) : "v"(L_CNT), "v"(1) : "memory");
        if (__builtin_amdgcn_readfirstlane(n) == 7) FLAGS(0xff, fso, tag, fkw);
    }
    };
    if (QPOS == 0) signal();
    mq(QFLAG, 22);
    int ln = lane;
    asm volatile("" : "+v"(ln));
    unsigned l16 = ln * 16, vcur = L_S0 + l16 + half * 4096, dvoff = l16, vwr = l16 + w4k, vxch = vwr ^ 16384, fvo = (ln & 7) * 8;
    asm volatile("" : "+v"(l16), "+v"(vcur), "+v"(dvoff), "+v"(vwr), "+v"(vxch), "+v"(fvo));
    SB_; mq(22, 23);
    if (QPOS == 23) signal();
    unsigned wvoff = (unsigned)((ln >> 3) * (D_ * 2) + ((ln & 7) ^ ((ln >> 2) & 4)) * 16);
    unsigned sidx = 0xdddddddd, vown = vwr;
    asm volatile("" : "+v"(wvoff), "+v"(sidx), "+v"(vown));
    SB_; mq(23, 24);
    if (QPOS == 24) signal();
    unsigned w0 = wbase + (4 * (ln >> 5) + ((ln & 15) >> 2)) * 128 + (ln & 3) * 8;
    asm volatile("" : "+v"(w0));
    SB_; mq(24, 25);
    if (QPOS == 25) signal();
    w0 |= (((ln >> 4) ^ kq) & 1) * 32 + (((kq & 2) * 32) ^ ((ln & 8) * 8));
    asm volatile("" : "+v"(w0));
    SB_; mq(25, 26);
    if (QPOS == 26) signal();
    unsigned w1 = w0 ^ 64, x0 = w0 + 1024 - (ln >> 5) * 512, x1 = x0 ^ 64, ooff = (ln & 31) * (D_ * 2) + (ln >> 5) * 16;
    asm volatile("" : "+v"(w1), "+v"(x0), "+v"(x1), "+v"(ooff));
    SB_; mq(26, 27);
    if (QPOS == 27) signal();
    u32x4 v10 = {}, v11;
    unsigned vr3 = L_XC + wid * 2048 + l16;
    asm volatile("" : "+v"(v10), "+v"(vr3));
    SB_; mq(27, 28);
    if (QPOS == 1) signal();
    unsigned narr;
    if (QPOS == 4) asm volatile("s_waitcnt vmcnt(0)\n\ts_mov_b64 exec, 1\n\tds_add_rtn_u32 %0, %1, %2\n\ts_mov_b64 exec, -1" : "=v"(narr) : "v"(L_CNT), "v"(1) : "memory");
    u32x4 q0, q1;
    float gsel;
    {
        const f32x16 q = aQ * QSCALE;
        q0 = pack_lo(q);
        f32x16 s = mfma32(k0, q0, f32x16{});
        q1 = pack_hi(q);
        if (hi) { q1[2] = 0u; q1[3] = 0u; }
        s = mfma32(k1, q1, s);
        SB_;
        if (QPOS == 4)
            asm volatile("s_waitcnt lgkmcnt(0)\n\tv_cmp_eq_u32 vcc, 7, %0\n\ts_bitcmp1_b32 vcc_lo, 0\n\ts_cselect_b64 exec, 0xff, 0\n\tglobal_store_dwordx2 %1, %2, %3 sc1\n\ts_mov_b64 exec, -1"
                         : "+v"(narr) : "v"(fso), "v"(tag), "s"(fkw) : "memory", "vcc", "scc");
        LAS char* qx = lds + L_XB + vown;
        lds_write16(qx, q0);
        const float glogit = upper_half(aQ[12]);
        const float gate = __builtin_amdgcn_rcpf(1.f + __expf(-glogit));
        gsel = (hb < 16) ? gate : 1.f - gate;
        asm volatile("" : "+v"(gsel));
        SB_;
        set_shift(q1, xhalf_max(max16(s)), hi);
        lds_write16(qx + 1024, q1);
    }
    u64 fv;
    if (QPOS == 3) signal();
    if (QPOS == 2) {
        asm volatile("s_waitcnt vmcnt(0) lgkmcnt(0)\n\ts_barrier" : "+v"(gsel) :: "memory");
        if (wid == 7) FLAGS(0xff, l16 * (FS / 16), tag, fkw);
    } else
    asm volatile("s_waitcnt lgkmcnt(0)\n\ts_barrier" : "+v"(gsel) :: "memory");
    f32x16 oa, ob;
    u32x16 ofa, ofb, ofc, ofd;
    {
        u32x4 qa0, qa1, k00, k01, k10, k11, v00, v01;
        unsigned sm0;
        asm volatile(ATTN_LOOP_ASM
            : [oa] "=&{v[192:207]}"(oa), [ob] "=&v"(ob), [qa0] "=&v"(qa0), [qa1] "=&v"(qa1), [k00] "=&v"(k00), [k01] "=&v"(k01), [k10] "=&v"(k10), [k11] "=&v"(k11), [v00] "=&v"(v00), [v01] "=&v"(v01), [v10] "+&v"(v10), [v11] "=&v"(v11),
              [vcur] "+&v"(vcur), [dvoff] "+&v"(dvoff), [vwr] "+&v"(vwr), [fv] "=&v"(fv), [cnt] "+&s"(cnt), [soff] "+&s"(soff), [sstr] "+&s"(sstr), [seen] "+&s"(seen), [sbit] "+&s"(sbit), [spin] "+&s"(spin), [seq] "+&s"(seq),
              "=&{v[208:223]}"(ofa), "=&{v[224:239]}"(ofb), "=&{v[240:255]}"(ofc), "=&{v[112:127]}"(ofd), [sm0] "=&s"(sm0), [swad] "+&s"(swad), [swao] "+&s"(swao), [ksrd] "+&{s[60:63]}"(kvsrd)
            : [qb0] "v"(q0), [qb1] "v"(q1), [idx] "v"(sidx), [wvoff] "v"(wvoff), [tag] "v"(tag), [fvo] "v"(fvo), [fptr] "s"(fkr),
              [wasrd] "s"(wasrd), [wbsrd] "{s[64:67]}"(wbsrd), [swb] "s"(swb), [vxch] "v"(vxch),
              [l16] "v"(l16), [osrd] "s"(osrd), [oso] "s"(oso), [sfl] "s"(sfl), [fw] "s"(fow), [fo] "s"(fr), [w4k] "s"(w4k), [vown] "v"(vown)
            : "memory", "scc", ATTN_LOOP_CLOB);
    }
    asm volatile("s_waitcnt lgkmcnt(3)" : "+{v[192:207]}"(oa));
    float l = upper_half(ob[12] + oa[12]);
    float sc = gsel * __builtin_amdgcn_rcpf(l);
    asm volatile("s_waitcnt lgkmcnt(0)" : "+{v[192:207]}"(oa), "+v"(sc));
    f32x16 o = ob + oa;
    {
        const f32x8 t8 = lo8(o) + hi8(o);
        const f32x4 t4 = __builtin_shufflevector(t8, t8, 0, 1, 2, 3) + __builtin_shufflevector(t8, t8, 4, 5, 6, 7);
        const f32x2 t2 = __builtin_shufflevector(t4, t4, 0, 1) + __builtin_shufflevector(t4, t4, 2, 3);
        const bool bad = !(l < 1.0e30f) | !(sc < 3.0e38f) | !(fabsf(t2[0] + t2[1]) < 3.0e38f);
        if (__builtin_expect(__any(bad), 0)) {
            if (hi) { q1[2] = 0u; q1[3] = 0u; }
            float m = -3.0e38f;
#pragma unroll 1
            for (int t = 0; t < 64; ++t) {
                const u32x4 a0 = __builtin_amdgcn_raw_buffer_load_b128(kvr, l16, t * 4096, 16), a1 = __builtin_amdgcn_raw_buffer_load_b128(kvr, l16 + 1024, t * 4096, 16);
                f32x16 s = mfma32(a0, q0, f32x16{});
                s = mfma32(a1, q1, s);
                m = fmaxf(m, max16(s));
            }
            set_shift(q1, xhalf_max(m), hi);
            o = f32x16{};
#pragma unroll 1
            for (int t = 0; t < 64; ++t) {
                const u32x4 a0 = __builtin_amdgcn_raw_buffer_load_b128(kvr, l16, t * 4096, 16), a1 = __builtin_amdgcn_raw_buffer_load_b128(kvr, l16 + 1024, t * 4096, 16);
                const u32x4 b0 = __builtin_amdgcn_raw_buffer_load_b128(kvr, l16 + 2048, t * 4096, 16), b1 = __builtin_amdgcn_raw_buffer_load_b128(kvr, l16 + 3072, t * 4096, 16);
                f32x16 s = mfma32(a0, q0, f32x16{});
                s = mfma32(a1, q1, s);
#pragma unroll
                for (int i = 0; i < 16; ++i) s[i] = __builtin_amdgcn_exp2f(s[i]);
                o = smfma32(b0, lo8(s), o, (int)0xdddddddd);
                o = smfma32(b1, hi8(s), o, (int)0xdddddddd);
            }
            o = o * (gsel * __builtin_amdgcn_rcpf(upper_half(o[12])));
            sc = 1.f;
#pragma unroll
            for (int i = 0; i < 16; ++i) {
                unsigned u = __float_as_uint(o[i]);
                asm volatile("" : "+v"(u));
                u = (u & 0x7fffffffu) > 0x7f800000u ? 0x7fc00000u : u;
                asm volatile("" : "+v"(u));
                o[i] = __uint_as_float(u);
            }
        }
    }
    {
        o = o * sc;
        u32x4 o0 = pack_lo(o), o1 = pack_hi(o);
        if (hi) { o1[2] = 0u; o1[3] = 0u; }
        asm volatile("buffer_store_dwordx4 %0, %2, %3, %4 offen sc1\n\tbuffer_store_dwordx4 %1, %2, %3, %4 offen offset:1024 sc1\n\ts_nop 1"
                     :: "v"(o0), "v"(o1), "v"(l16), "s"(osrd), "s"(oso) : "memory");
    }
    if (idx < 28) {
        f32x16 acc0, acc1;
        unsigned t, wd; u64 ret;
        asm volatile(P3_ASM
            : [a0] "=&v"(acc0), [a1] "=&v"(acc1), [t] "=&v"(t), "+{v[208:223]}"(ofa), "+{v[224:239]}"(ofb), "+{v[240:255]}"(ofc), "+{v[112:127]}"(ofd), [wd] "=&s"(wd), [ret] "=&s"(ret), [spin] "+&s"(spin)
            : [l16] "v"(l16), [osrd] "s"(osrd), [so0] "s"(so0), [so1] "s"(so1), [so2] "s"(so2), [so3] "s"(so3),
              [w0] "v"(w0), [w1] "v"(w1), [x0] "v"(x0), [x1] "v"(x1)
            : "memory", "scc", P3_CLOB);
        auto q4 = [](const f32x16& a, int g) { return (f32x4){a[4 * g], a[4 * g + 1], a[4 * g + 2], a[4 * g + 3]}; };
        LAS char* r12 = lds + vown;
        LAS char* r3 = lds + vr3;
        unsigned a1 = vown ^ 8192, a3 = vr3 + ((kq ^ 3) - kq) * 4096;
        asm volatile("" : "+v"(a1), "+v"(a3));
        *(LAS f32x4*)(r12 + 2048) = q4(acc1, 0); *(LAS f32x4*)(r12 + 3072) = q4(acc1, 1);
        *(LAS f32x4*)r3 = q4(acc1, 2); *(LAS f32x4*)(r3 + 1024) = q4(acc1, 3);
        *(LAS f32x4*)r12 = q4(acc0, 2); *(LAS f32x4*)(r12 + 1024) = q4(acc0, 3);
        asm volatile("s_waitcnt lgkmcnt(0)\n\ts_barrier" ::: "memory");
        {
            const LAS char* p1 = lds + a1;
            const LAS char* p2 = lds + vxch + 2048;
            const LAS char* p3 = lds + a3;
            const f32x4 t0 = *(const LAS f32x4*)p1, t1 = *(const LAS f32x4*)(p1 + 1024), t2 = *(const LAS f32x4*)p2, t3 = *(const LAS f32x4*)p3;
            const f32x4 t4 = *(const LAS f32x4*)(p2 + 1024), t5 = *(const LAS f32x4*)(p3 + 1024);
            SB_;
            const f32x4 a = (q4(acc0, 0) + t0) + (t2 + t3), b = (q4(acc0, 1) + t1) + (t4 + t5);
            const auto e = __builtin_amdgcn_permlane32_swap(cvtpk(a[0], a[1]), cvtpk(b[0], b[1]), false, false);
            const auto f = __builtin_amdgcn_permlane32_swap(cvtpk(a[2], a[3]), cvtpk(b[2], b[3]), false, false);
            *(GAS u32x4*)((GAS char*)outp + ooff) = (u32x4){e[0], f[0], e[1], f[1]};
        }
        if (blockIdx.x == 0 && vown == 0) __hip_atomic_store((GAS u64*)(ws + WS_EPOCH), tag, RLX_AGENT);
    }
}

extern "C" void kernel_launch(void* const* d_in, const int* in_sizes, int n_in, void* d_out, int out_size, void* d_ws, size_t ws_size, hipStream_t stream) {
    (void)hipFuncSetAttribute((const void*)k_fused, hipFuncAttributeMaxDynamicSharedMemorySize, LDS_BYTES);
    k_fused<<<256, 512, LDS_BYTES, stream>>>((const uint16_t*)d_in[0], (const uint16_t*)d_in[1], (const uint16_t*)d_in[4], (const uint16_t*)d_in[2], (const uint16_t*)d_in[5],
                                             (const uint16_t*)d_in[6], (uint16_t*)d_out, (char*)d_ws);
}
```
